```python
import torch

def _softmax(x, axis=-1):
    return torch.softmax(x, dim=axis)
def _gelu(x):
    return torch.nn.functional.gelu(x, approximate="tanh")
def _silu(x):
    return torch.nn.functional.silu(x)
def _sigmoid(x):
    return torch.sigmoid(x)
def _layernorm(x, axis=-1, eps=1e-5):
    m = x.mean(dim=axis, keepdim=True)
    v = x.var(dim=axis, keepdim=True, unbiased=False)
    return (x - m) / torch.sqrt(v + eps)
def _scatter_add(tgt, idx, src, axis=0):
    return tgt.clone().index_add_(axis, idx, src)
def _segment_sum(src, seg, n):
    out = torch.zeros((n,) + src.shape[1:], dtype=src.dtype)
    return out.index_add_(0, seg, src)
def _pad(x, pw):
    flat = tuple(v for pair in reversed(pw) for v in pair)
    return torch.nn.functional.pad(x, flat)

kernel_name = 'parallel_sum__sliding_block_cached__dense_activation_silu__decode'

def setup_inputs():

    _w = (lambda a: (lambda t: t.to(torch.bfloat16) if t.is_floating_point() else t)(torch.as_tensor(a)))
    def _A_setup():
        B, Sq, D = 1, 1, 512
        Hq, Hkv, Sk, Dh = 32, 8, 65536, 128
        assert Hq % Hkv == 0, (Hq, Hkv)
        return {
            "x": _w((lambda *s: torch.randn(*s, dtype=torch.bfloat16) * 0.05)(B, Sq, D)),
            "k_cache": _w((lambda *s: torch.randn(*s, dtype=torch.bfloat16) * 3.2)(B, Hkv, Sk, Dh)),
            "v_cache": _w((lambda *s: torch.randn(*s, dtype=torch.bfloat16) * 0.8)(B, Hkv, Sk, Dh)),
            "wq": _w((lambda *s: torch.randn(*s, dtype=torch.bfloat16) * 3.2)(D, Hq * Dh) * (D ** -0.5)),
            "wo": _w((lambda *s: torch.randn(*s, dtype=torch.bfloat16) * 0.8)(Hq * Dh, D) * ((Hq * Dh) ** -0.5)),
        }
    _A_in = _A_setup()

    def _B_setup():
        B, S, D, F = 1, 1, 512, 1792
        return {
            "x": _w((lambda *s: torch.randn(*s, dtype=torch.bfloat16) * 0.05)(B, S, D)),
            "w1": _w((lambda *s: torch.randn(*s, dtype=torch.bfloat16) * 3.2)(D, F) * (D ** -0.5)),
            "w2": _w((lambda *s: torch.randn(*s, dtype=torch.bfloat16) * 0.4)(F, D) * (F ** -0.5)),
        }
    _B_in = _B_setup()

    return {
        "A_x": _A_in["x"],
        "A_k_cache": _A_in["k_cache"],
        "A_v_cache": _A_in["v_cache"],
        "A_wq": _A_in["wq"],
        "A_wo": _A_in["wo"],
        "B_x": _B_in["x"],
        "B_w1": _B_in["w1"],
        "B_w2": _B_in["w2"],
    }

def reference(A_x, A_k_cache, A_v_cache, A_wq, A_wo, B_x, B_w1, B_w2):
    def _A_ref(x, k_cache, v_cache, wq, wo):
        B, Sq, D = x.shape
        Hq, Dh = 32, 128
        Hkv, Sk = k_cache.shape[1], k_cache.shape[2]
        W = min(4096, max(256, Sk // 4))
        rep = Hq // Hkv
        q = (lambda x, *d: x.permute(*d))((x @ wq).reshape(B, Sq, Hq, Dh), 0, 2, 1, 3)
        k = (lambda x, n, axis: torch.repeat_interleave(x, n, dim=axis))(k_cache[:, :, Sk - W:, :], rep, axis=1)
        v = (lambda x, n, axis: torch.repeat_interleave(x, n, dim=axis))(v_cache[:, :, Sk - W:, :], rep, axis=1)
        scores = torch.einsum("bhqd,bhkd->bhqk", q, k) * (Dh ** -0.5)
        p = _softmax(scores, axis=-1)
        o = torch.einsum("bhqk,bhkd->bhqd", p, v)
        return (lambda x, *d: x.permute(*d))(o, 0, 2, 1, 3).reshape(B, Sq, Hq * Dh) @ wo

    def _B_ref(x, w1, w2):
        h = torch.einsum("bsd,df->bsf", x, w1)
        h = _silu(h)
        out = torch.einsum("bsf,fd->bsd", h, w2)
        return out

    x = A_x
    xn = _layernorm(x, axis=-1)
    return x + _A_ref(xn, A_k_cache, A_v_cache, A_wq, A_wo) + _B_ref(xn, B_w1, B_w2)
```

```cpp
#include <hip/hip_runtime.h>
#include <hip/hip_bf16.h>
#include <stdint.h>

typedef unsigned short u16;
__device__ __forceinline__ float bf2f(u16 b) { return __uint_as_float(((unsigned)b) << 16); }
__device__ __forceinline__ u16 f2bf(float f) { __hip_bfloat16 h = __float2bfloat16(f); return *(u16*)&h; }
__device__ __forceinline__ float rbf(float f) { return bf2f(f2bf(f)); }

constexpr int D = 512, HQ = 32, HKV = 8, SK = 65536, DH = 128, W = 4096, F = 1792;

__device__ void layernorm(const u16* x, float* xn  , float* red) {
  int t = threadIdx.x;
  float a = bf2f(x[t]), b = bf2f(x[t + 256]);
  red[t] = a + b; __syncthreads();
  for (int s = 128; s > 0; s >>= 1) { if (t < s) red[t] += red[t + s]; __syncthreads(); }
  float mean = red[0] / 512.f; __syncthreads();
  float da = a - mean, db = b - mean;
  red[t] = da * da + db * db; __syncthreads();
  for (int s = 128; s > 0; s >>= 1) { if (t < s) red[t] += red[t + s]; __syncthreads(); }
  float var = red[0] / 512.f; __syncthreads();
  float m = rbf(mean), v = rbf(var);
  float den = rbf(sqrtf(rbf(v + 1e-5f)));
  xn[t] = rbf(rbf(a - m) / den);
  xn[t + 256] = rbf(rbf(b - m) / den);
  __syncthreads();
}

__global__ void k_proj(const u16* x, const u16* wq, const u16* w1, float* q, float* h) {
  __shared__ float xn[512]; __shared__ float red[256];
  layernorm(x, xn, red);
  int c = blockIdx.x * 256 + threadIdx.x;
  if (c < 4096) {
    float acc = 0;
    for (int i = 0; i < 512; ++i) acc += xn[i] * bf2f(wq[(size_t)i * 4096 + c]);
    q[c] = rbf(acc);
  } else {
    c -= 4096;
    float acc = 0;
    for (int i = 0; i < 512; ++i) acc += xn[i] * bf2f(w1[(size_t)i * F + c]);
    float hh = rbf(acc);
    h[c] = rbf(hh / (1.f + expf(-hh)));
  }
}

__global__ void k_attn(const float* q, const u16* K, const u16* V, float* o) {
  __shared__ float s[W]; __shared__ float red[256]; __shared__ float qs[128];
  int hq = blockIdx.x, g = hq / 4, t = threadIdx.x;
  if (t < 128) qs[t] = q[hq * 128 + t];
  __syncthreads();
  const u16* Kb = K + ((size_t)g * SK + (SK - W)) * DH;
  const u16* Vb = V + ((size_t)g * SK + (SK - W)) * DH;
  float mx = -INFINITY;
  for (int k = t; k < W; k += 256) {
    float acc = 0;
    for (int d = 0; d < 128; ++d) acc += qs[d] * bf2f(Kb[(size_t)k * 128 + d]);
    float sc = rbf(rbf(acc) * 0.08838834764831845f);
    s[k] = sc; mx = fmaxf(mx, sc);
  }
  red[t] = mx; __syncthreads();
  for (int st = 128; st > 0; st >>= 1) { if (t < st) red[t] = fmaxf(red[t], red[t + st]); __syncthreads(); }
  mx = red[0]; __syncthreads();
  float sum = 0;
  for (int k = t; k < W; k += 256) { float e = expf(s[k] - mx); s[k] = e; sum += e; }
  red[t] = sum; __syncthreads();
  for (int st = 128; st > 0; st >>= 1) { if (t < st) red[t] += red[t + st]; __syncthreads(); }
  sum = red[0]; __syncthreads();
  for (int k = t; k < W; k += 256) s[k] = rbf(s[k] / sum);
  __syncthreads();
  int d = t & 127, half = t >> 7;
  float acc = 0;
  for (int k = half; k < W; k += 2) acc += s[k] * bf2f(Vb[(size_t)k * 128 + d]);
  red[t] = acc; __syncthreads();
  if (t < 128) o[hq * 128 + t] = rbf(red[t] + red[t + 128]);
}

__global__ void k_out(const u16* x, const float* o, const float* h, const u16* wo, const u16* w2, u16* out) {
  __shared__ float red[256];
  int c = blockIdx.x, t = threadIdx.x;
  float a = 0, b = 0;
  for (int i = t; i < 4096; i += 256) a += o[i] * bf2f(wo[(size_t)i * 512 + c]);
  for (int i = t; i < F; i += 256) b += h[i] * bf2f(w2[(size_t)i * 512 + c]);
  red[t] = a; __syncthreads();
  for (int st = 128; st > 0; st >>= 1) { if (t < st) red[t] += red[t + st]; __syncthreads(); }
  a = red[0]; __syncthreads();
  red[t] = b; __syncthreads();
  for (int st = 128; st > 0; st >>= 1) { if (t < st) red[t] += red[t + st]; __syncthreads(); }
  b = red[0];
  if (t == 0) out[c] = f2bf(rbf(bf2f(x[c]) + rbf(a)) + rbf(b));
}

extern "C" void kernel_launch(void* const* d_in, const int* in_sizes, int n_in,
                              void* d_out, int out_size, void* d_ws, size_t ws_size,
                              hipStream_t stream) {
  const u16* x = (const u16*)d_in[0];
  const u16* K = (const u16*)d_in[1];
  const u16* V = (const u16*)d_in[2];
  const u16* wq = (const u16*)d_in[3];
  const u16* wo = (const u16*)d_in[4];
  const u16* w1 = (const u16*)d_in[6];
  const u16* w2 = (const u16*)d_in[7];
  float* q = (float*)d_ws; float* h = q + 4096; float* o = h + 2048;
  k_proj<<<(4096 + F) / 256, 256, 0, stream>>>(x, wq, w1, q, h);
  k_attn<<<32, 256, 0, stream>>>(q, K, V, o);
  k_out<<<512, 256, 0, stream>>>(x, o, h, wo, w2, (u16*)d_out);
}
```

```cpp
#include <hip/hip_runtime.h>
#include <stdint.h>

#define DELTA 8
#ifndef NV_EARLY
#define NV_EARLY 0
#endif
#define CDELTA (DELTA + 2)

typedef unsigned short u16;
typedef unsigned long long u64;
typedef unsigned v4u __attribute__((ext_vector_type(4)));
typedef unsigned v2u __attribute__((ext_vector_type(2)));
typedef float f32x4 __attribute__((ext_vector_type(4)));
typedef float f32x2 __attribute__((ext_vector_type(2)));
typedef __bf16 bf16x2 __attribute__((ext_vector_type(2)));
typedef __bf16 bf16x8 __attribute__((ext_vector_type(8)));
typedef short s16x4 __attribute__((ext_vector_type(4)));
#define DI __device__ __forceinline__
#define GAS __attribute__((address_space(1)))
#define LAS __attribute__((address_space(3)))

constexpr int SK = 65536, WIN = 4096;
constexpr int AUX_NT = 2, AUX_SC1 = 16;
constexpr int WS_QG = 4096, WS_HP = WS_QG + 65536, WS_P2 = WS_HP + 32768, WS_P3 = WS_P2 + 524288, WS_END = WS_P3 + 262144;
constexpr unsigned SPIN_CAP = 1u << 18;
constexpr int L_QP = 0, L_HP = 512, L_QS = 1024, L_ST = L_QS + 4 * 272, L_TAG = L_ST + 128, L_ML = 2304, L_END = L_ML + 8192;

DI float asf(unsigned u) { return __builtin_bit_cast(float, u); }
DI unsigned asu(float f) { return __builtin_bit_cast(unsigned, f); }
DI unsigned pk(float lo, float hi) { f32x2 v = {lo, hi}; return __builtin_bit_cast(unsigned, __builtin_convertvector(v, bf16x2)); }
DI float lo16(unsigned u) { return asf(u << 16); }
DI float hi16(unsigned u) { return asf(u & 0xffff0000u); }
DI float rbf(float f) { return lo16(pk(f, f)); }
DI unsigned perm_lo(unsigned a, unsigned b) { return __builtin_amdgcn_perm(b, a, 0x05040100u); }
DI unsigned perm_hi(unsigned a, unsigned b) { return __builtin_amdgcn_perm(b, a, 0x07060302u); }
DI float dot2(unsigned a, unsigned b, float c) { return __builtin_amdgcn_fdot2_f32_bf16(__builtin_bit_cast(bf16x2, a), __builtin_bit_cast(bf16x2, b), c, false); }
template <int CTRL> DI float dpp(float x) { return asf(__builtin_amdgcn_mov_dpp(asu(x), CTRL, 0xf, 0xf, true)); }
template <int CTRL> DI unsigned dppu(unsigned x) { return __builtin_amdgcn_mov_dpp(x, CTRL, 0xf, 0xf, true); }
constexpr int ROR1 = 0x121, ROR2 = 0x122, ROR4 = 0x124, ROR8 = 0x128, XOR1 = 0xB1;
DI float sw32add(float a, float b) { auto r = __builtin_amdgcn_permlane32_swap(asu(a), asu(b), false, false); return asf(r[0]) + asf(r[1]); }
DI float sw16add(float a, float b) { auto r = __builtin_amdgcn_permlane16_swap(asu(a), asu(b), false, false); return asf(r[0]) + asf(r[1]); }
DI float sw32max(float a) { auto r = __builtin_amdgcn_permlane32_swap(asu(a), asu(a), false, false); return fmaxf(asf(r[0]), asf(r[1])); }
DI float sw16max(float a) { auto r = __builtin_amdgcn_permlane16_swap(asu(a), asu(a), false, false); return fmaxf(asf(r[0]), asf(r[1])); }
DI float wave_sum(float x) { x += dpp<ROR8>(x); x += dpp<ROR4>(x); x += dpp<ROR2>(x); x += dpp<ROR1>(x); x = sw16add(x, x); return sw32add(x, x); }
DI f32x4 mfma32(v4u a, v4u b, f32x4 c) { return __builtin_amdgcn_mfma_f32_16x16x32_bf16(__builtin_bit_cast(bf16x8, a), __builtin_bit_cast(bf16x8, b), c, 0, 0, 0); }
DI f32x4 mfma16(v2u a, v2u b, f32x4 c) { return __builtin_amdgcn_mfma_f32_16x16x16bf16_1k(__builtin_bit_cast(s16x4, a), __builtin_bit_cast(s16x4, b), c, 0, 0, 0); }
DI __amdgpu_buffer_rsrc_t rsrc(const void* p, int bytes) { return __builtin_amdgcn_make_buffer_rsrc((void*)p, 0, bytes, 0x00020000); }
DI void barrier() { asm volatile("s_waitcnt lgkmcnt(0)\n\ts_barrier" ::: "memory"); }
DI void fence_sched() { __builtin_amdgcn_sched_barrier(0); }
DI unsigned max3u(unsigned a, unsigned b, unsigned c) { return max(max(a, b), c); }
DI unsigned max4(v4u v) { return max(max(v.x, v.y), max(v.z, v.w)); }
DI unsigned canon(unsigned v) { return min(v, 0xfffffffeu); }

#define STAMP(i)
#define STAMP_DECL
#define STAMP_OUT()

DI f32x2 reduce8(const float (&a)[8]) {
  float t0 = sw32add(a[0], a[4]), t1 = sw32add(a[1], a[5]), t2 = sw32add(a[2], a[6]), t3 = sw32add(a[3], a[7]);
  float u0 = sw16add(t0, t2), u1 = sw16add(t1, t3);
  u0 += dpp<ROR8>(u0); u1 += dpp<ROR8>(u1); u0 += dpp<ROR4>(u0); u1 += dpp<ROR4>(u1);
  return f32x2{u0, u1};
}

__global__ void __launch_bounds__(320, 2)
fused_decode(const u16* __restrict__ x, const u16* __restrict__ Kc, const u16* __restrict__ Vc, const u16* __restrict__ wq, const u16* __restrict__ wo,
             const u16* __restrict__ w1, const u16* __restrict__ w2, u16* __restrict__ out, unsigned char* ws) {
  __shared__ __attribute__((aligned(16))) unsigned char lds_[L_END];
  LAS unsigned char* L = (LAS unsigned char*)lds_;
  STAMP_DECL;
  const int b = blockIdx.x, g = b & 7, s = b >> 3;
  const int l = threadIdx.x & 63, wave = __builtin_amdgcn_readfirstlane(threadIdx.x >> 6);
  const int c = l & 15, h = l >> 4;
  STAMP(0);
  const __amdgpu_buffer_rsrc_t rw = rsrc(ws, WS_END);
  asm volatile("" :: "s"(x), "s"(Kc), "s"(Vc), "s"(wq), "s"(wo), "s"(w1), "s"(w2), "s"(out), "s"(ws));

  if (wave != 0) {
    const int a = wave - 1, u = l >> 2, cq = l & 3;
    const __amdgpu_buffer_rsrc_t rx = rsrc(x, 1024), rq = rsrc(wq, 512 * 4096 * 2), r1 = rsrc(w1, s < 28 ? 512 * 1792 * 2 : 0);
    const size_t kvoff = ((size_t)g * SK + (SK - WIN) + s * 128 + a * 32) * 128;
    const __amdgpu_buffer_rsrc_t rk = rsrc(Kc + kvoff, 8192), rv = rsrc(Vc + kvoff, 8192);
    const int R0 = 256 * (s & 1) + 64 * a + 4 * u, R1 = 128 * (s & 3) + 32 * a + 2 * u;
    const v4u xf = __builtin_amdgcn_raw_buffer_load_b128(rx, l * 16, 0, 0);
    const v2u x4 = __builtin_amdgcn_raw_buffer_load_b64(rx, R0 * 2, 0, 0);
    const unsigned x2 = __builtin_amdgcn_raw_buffer_load_b32(rx, R1 * 2, 0, 0);
    v4u wqr[4], w1r[2], kr[8], vr[8];
    const int qoff = (R0 * 4096 + 512 * g + 32 * (s >> 1) + 8 * cq) * 2;
#pragma unroll
    for (int i = 0; i < 4; ++i) wqr[i] = __builtin_amdgcn_raw_buffer_load_b128(rq, qoff, i * 8192, AUX_NT);
    const int off1 = (R1 * 1792 + 224 * g + 32 * (s >> 2) + 8 * cq) * 2;
#pragma unroll
    for (int i = 0; i < 2; ++i) w1r[i] = __builtin_amdgcn_raw_buffer_load_b128(r1, off1, i * 3584, AUX_NT);
#pragma unroll
    for (int i = 0; i < 8; ++i) kr[i] = __builtin_amdgcn_raw_buffer_load_b128(rk, c * 256 + h * 16 + (i & 3) * 64, (i >> 2) * 4096, AUX_NT);
#pragma unroll
    for (int j = 0; j < NV_EARLY; ++j) vr[j] = __builtin_amdgcn_raw_buffer_load_b128(rv, h * 1024 + c * 16 + (j & 3) * 256, (j >> 2) * 4096, AUX_NT);
    fence_sched();
    STAMP(1);
    float xv[8];
#pragma unroll
    for (int i = 0; i < 4; ++i) { xv[2 * i] = lo16(xf[i]); xv[2 * i + 1] = hi16(xf[i]); }
    float sm = ((xv[0] + xv[1]) + (xv[2] + xv[3])) + ((xv[4] + xv[5]) + (xv[6] + xv[7]));
    const float mean = wave_sum(sm) * (1.f / 512.f);
    float sq = 0.f;
#pragma unroll
    for (int i = 0; i < 8; ++i) { const float d = xv[i] - mean; sq = fmaf(d, d, sq); }
    const float var = wave_sum(sq) * (1.f / 512.f);
    const float mb = rbf(mean);
    const float rden = __builtin_amdgcn_rcpf(rbf(__builtin_amdgcn_sqrtf(rbf(rbf(var) + 1e-5f))));
    auto norm2 = [&](unsigned p) { const unsigned d = pk(lo16(p) - mb, hi16(p) - mb); return pk(lo16(d) * rden, hi16(d) * rden); };
    const unsigned xq0 = norm2(x4.x), xq1 = norm2(x4.y), xh = norm2(x2);
    {
      float acc[8];
#pragma unroll
      for (int t = 0; t < 4; ++t) {
        acc[2 * t] = dot2(perm_lo(wqr[2][t], wqr[3][t]), xq1, dot2(perm_lo(wqr[0][t], wqr[1][t]), xq0, 0.f));
        acc[2 * t + 1] = dot2(perm_hi(wqr[2][t], wqr[3][t]), xq1, dot2(perm_hi(wqr[0][t], wqr[1][t]), xq0, 0.f));
      }
      const f32x2 r = reduce8(acc);
      if ((l & 12) == 0) *(LAS f32x2*)(L + L_QP + a * 128 + (8 * cq + 2 * h) * 4) = r;
    }
    STAMP(2);
    barrier();
    {
      float acc[8];
#pragma unroll
      for (int t = 0; t < 4; ++t) {
        acc[2 * t] = dot2(perm_lo(w1r[0][t], w1r[1][t]), xh, 0.f);
        acc[2 * t + 1] = dot2(perm_hi(w1r[0][t], w1r[1][t]), xh, 0.f);
      }
      const f32x2 r = reduce8(acc);
      if ((l & 12) == 0) *(LAS f32x2*)(L + L_HP + a * 128 + (8 * cq + 2 * h) * 4) = r;
    }
    barrier();
    if (DELTA) __builtin_amdgcn_s_sleep(DELTA);
    STAMP(3);
    const v2u tg = *(const LAS v2u*)(L + L_TAG);
    const unsigned tag_lo = tg.x, tag_hi = tg.y;
    const int goff = WS_QG + (g * 1024 + 128 * a + 2 * l) * 8;
    v4u g0 = __builtin_amdgcn_raw_buffer_load_b128(rw, goff, 0, AUX_SC1);
    v4u g1 = __builtin_amdgcn_raw_buffer_load_b128(rw, goff, 4096, AUX_SC1);
#pragma unroll
    for (int j = NV_EARLY; j < 8; ++j) vr[j] = __builtin_amdgcn_raw_buffer_load_b128(rv, h * 1024 + c * 16 + (j & 3) * 256, (j >> 2) * 4096, AUX_NT);
    fence_sched();
    auto qbad = [&](v4u p, v4u q) {
      const unsigned hi = (p.y ^ tag_hi) | (p.w ^ tag_hi) | (q.y ^ tag_hi) | (q.w ^ tag_hi);
      const unsigned lo = ((p.x ^ tag_lo) | (p.z ^ tag_lo) | (q.x ^ tag_lo) | (q.z ^ tag_lo)) & 0xffu;
      return __builtin_amdgcn_ballot_w64((hi | lo) != 0u) != 0ull; };
    if (__builtin_expect(qbad(g0, g1), 0)) {
      STAMP(9);
      for (unsigned sp = 0; sp < SPIN_CAP; ++sp) {
        asm volatile("" ::: "memory");
        g0 = __builtin_amdgcn_raw_buffer_load_b128(rw, goff, 0, AUX_SC1);
        g1 = __builtin_amdgcn_raw_buffer_load_b128(rw, goff, 4096, AUX_SC1);
        if (!qbad(g0, g1)) break;
      }
    }
    *(LAS unsigned*)(L + L_QS + a * 272 + l * 4) = pk(asf(g0.x & 0xffffff00u) + asf(g1.x & 0xffffff00u), asf(g0.z & 0xffffff00u) + asf(g1.z & 0xffffff00u));
    STAMP(4);
    barrier();
    f32x4 sc[2] = {{0.f, 0.f, 0.f, 0.f}, {0.f, 0.f, 0.f, 0.f}};
#pragma unroll
    for (int t = 0; t < 4; ++t) {
      const v4u qf = *(const LAS v4u*)(L + L_QS + (c & 3) * 272 + t * 64 + h * 16);
      sc[0] = mfma32(kr[t], qf, sc[0]);
      sc[1] = mfma32(kr[4 + t], qf, sc[1]);
    }
    float sv[8];
#pragma unroll
    for (int i = 0; i < 4; ++i) {
      const unsigned r1_ = pk(sc[i >> 1][2 * (i & 1)], sc[i >> 1][2 * (i & 1) + 1]);
      const unsigned r2_ = pk(lo16(r1_) * 0.08838834764831845f, hi16(r1_) * 0.08838834764831845f);
      sv[2 * i] = lo16(r2_); sv[2 * i + 1] = hi16(r2_);
    }
    float m = fmaxf(fmaxf(fmaxf(sv[0], sv[1]), fmaxf(sv[2], sv[3])), fmaxf(fmaxf(sv[4], sv[5]), fmaxf(sv[6], sv[7])));
    m = sw32max(sw16max(m));
    constexpr float LOG2E = 1.4426950408889634f;
    float p[8], ls = 0.f;
#pragma unroll
    for (int i = 0; i < 8; ++i) { p[i] = __builtin_amdgcn_exp2f((sv[i] - m) * LOG2E); ls += p[i]; }
    ls = sw16add(ls, ls); ls = sw32add(ls, ls);
    if (l < 4) *(LAS f32x2*)(L + L_ST + l * 32 + a * 8) = f32x2{m * LOG2E, ls};
    const v4u pf = {pk(p[0], p[1]), pk(p[2], p[3]), pk(p[4], p[5]), pk(p[6], p[7])};
    STAMP(5);
    f32x4 oa[2] = {{0.f, 0.f, 0.f, 0.f}, {0.f, 0.f, 0.f, 0.f}};
#pragma unroll
    for (int ga = 0; ga < 4; ++ga) {
      const bool on = (c >> 2) == ga;
      const v4u pm = {on ? pf.x : 0u, on ? pf.y : 0u, on ? pf.z : 0u, on ? pf.w : 0u};
      const v4u v0 = {perm_lo(vr[0][ga], vr[1][ga]), perm_lo(vr[2][ga], vr[3][ga]), perm_lo(vr[4][ga], vr[5][ga]), perm_lo(vr[6][ga], vr[7][ga])};
      const v4u v1 = {perm_hi(vr[0][ga], vr[1][ga]), perm_hi(vr[2][ga], vr[3][ga]), perm_hi(vr[4][ga], vr[5][ga]), perm_hi(vr[6][ga], vr[7][ga])};
      oa[0] = mfma32(v0, pm, oa[0]);
      oa[1] = mfma32(v1, pm, oa[1]);
    }
    *(LAS f32x4*)(L + L_ML + a * 2048 + l * 16) = oa[0];
    *(LAS f32x4*)(L + L_ML + a * 2048 + 1024 + l * 16) = oa[1];
    STAMP(6);
    barrier();
    {
      const int qd = (l >> 3) & 3, kap = l & 7;
      const f32x4 s01 = *(const LAS f32x4*)(L + L_ST + a * 32), s23 = *(const LAS f32x4*)(L + L_ST + a * 32 + 16);
      const int src = L_ML + (kap & 1) * 1024 + (4 * (kap >> 1) + a + 16 * qd) * 16;
      const f32x4 o0 = *(const LAS f32x4*)(L + src), o1 = *(const LAS f32x4*)(L + src + 2048), o2 = *(const LAS f32x4*)(L + src + 4096), o3 = *(const LAS f32x4*)(L + src + 6144);
      const float M = fmaxf(fmaxf(s01.x, s01.z), fmaxf(s23.x, s23.z));
      float w0 = __builtin_amdgcn_exp2f(s01.x - M), w1_ = __builtin_amdgcn_exp2f(s01.z - M), w2_ = __builtin_amdgcn_exp2f(s23.x - M), w3 = __builtin_amdgcn_exp2f(s23.z - M);
      const float Ls = (w0 * s01.y + w1_ * s01.w) + (w2_ * s23.y + w3 * s23.w);
      const float inv = __builtin_amdgcn_rcpf(Ls);
      const unsigned lse = asu(M + __builtin_amdgcn_logf(Ls));
      w0 *= inv; w1_ *= inv; w2_ *= inv; w3 *= inv;
      const f32x4 o = (o0 * w0 + o1 * w1_) + (o2 * w2_ + o3 * w3);
      const v4u pv = {canon(__builtin_amdgcn_perm(asu(o.x), lse, 0x07060500u)), canon(__builtin_amdgcn_perm(asu(o.y), lse, 0x07060501u)),
                      canon(__builtin_amdgcn_perm(asu(o.z), lse, 0x07060502u)), canon(__builtin_amdgcn_perm(asu(o.w), lse, 0x07060503u))};
      if (l < 32) __builtin_amdgcn_raw_buffer_store_b128(pv, rw, WS_P2 + (((4 * g + a) * 4 + qd) * 32 + s) * 128 + kap * 16, 0, AUX_SC1);
    }
    STAMP(7);
  } else {
    const int rgl = s >> 1, ch = s & 1, rg = 16 * g + rgl;
    const v2u ep = __builtin_amdgcn_raw_buffer_load_b64(rw, 0, 0, 0);
    const v4u ones = {~0u, ~0u, ~0u, ~0u};
    __builtin_amdgcn_raw_buffer_store_b128(ones, rw, WS_P2 + ((16 * g + (l >> 3)) * 32 + s) * 128 + (l & 7) * 16, 0, AUX_SC1);
    __builtin_amdgcn_raw_buffer_store_b128(ones, rw, WS_P2 + ((16 * g + 8 + (l >> 3)) * 32 + s) * 128 + (l & 7) * 16, 0, AUX_SC1);
    __builtin_amdgcn_raw_buffer_store_b128(ones, rw, WS_P3 + (ch * 128 + rg) * 1024 + l * 16, 0, AUX_SC1);
    const int hpoff = WS_HP + (g * 4 + (s & 3)) * 1024 + (s >> 2) * 128;
    if (l < 8) __builtin_amdgcn_raw_buffer_store_b128(ones, rw, hpoff + l * 16, 0, AUX_SC1);
    asm volatile("s_waitcnt vmcnt(0)" ::: "memory");
    const u64 tag = (((u64)ep.y << 32) | ep.x) + 1;
    const unsigned tag_lo = (unsigned)tag & 0xffu, tag_hi = (unsigned)(tag >> 8);
    *(LAS v2u*)(L + L_TAG) = v2u{tag_lo, tag_hi};
    STAMP(1);
    barrier();
    {
      const LAS float* qp = (const LAS float*)(L + L_QP) + (l & 31);
      const float q = (qp[0] + qp[32]) + (qp[64] + qp[96]);
      const v2u gr = {(asu(q) & 0xffffff00u) | tag_lo, tag_hi};
      if (l < 32) __builtin_amdgcn_raw_buffer_store_b64(gr, rw, WS_QG + ((g * 2 + (s & 1)) * 512 + 32 * (s >> 1) + l) * 8, 0, AUX_SC1);
    }
    STAMP(2);
    barrier();
    {
      const LAS float* hp = (const LAS float*)(L + L_HP) + (l & 31);
      const float hv = (hp[0] + hp[32]) + (hp[64] + hp[96]);
      if (l < 32) __builtin_amdgcn_raw_buffer_store_b32(canon(asu(hv)), rw, hpoff + l * 4, 0, AUX_SC1);
    }
    if (CDELTA) __builtin_amdgcn_s_sleep(CDELTA);
    STAMP(3);
    const __amdgpu_buffer_rsrc_t ro = rsrc(wo, 4096 * 512 * 2), r2 = rsrc(w2, 1792 * 512 * 2), rx = rsrc(x, 1024);
    v4u wr[2][8], fr[2][4];
    const int ooff = ((32 * rg + 2 * h) * 512 + 256 * ch + 8 * c) * 2;
#pragma unroll
    for (int j = 0; j < 8; ++j)
#pragma unroll
      for (int cb = 0; cb < 2; ++cb) wr[cb][j] = __builtin_amdgcn_raw_buffer_load_b128(ro, ooff + cb * 256, (8 * (j & 3) + (j >> 2)) * 1024, AUX_NT);
#pragma unroll
    for (int j = 0; j < 4; ++j)
#pragma unroll
      for (int cb = 0; cb < 2; ++cb) fr[cb][j] = __builtin_amdgcn_raw_buffer_load_b128(r2, ((14 * rg + min(4 * h + j, 13)) * 512 + 256 * ch + 8 * c) * 2 + cb * 256, 0, AUX_NT);
    const int rpos = 4 * (b & 15) + (l & 3);
    const int rcol = 256 * ((b >> 4) & 1) + 128 * (rpos >> 5) + 8 * (rpos & 15) + 4 * ((rpos >> 4) & 1) + h;
    const unsigned xres = __builtin_amdgcn_raw_buffer_load_b16(rx, rcol * 2, 0, 0);
    fence_sched();
    STAMP(4);
    barrier();
    barrier();
    STAMP(5);
    f32x4 acc[4] = {{0.f, 0.f, 0.f, 0.f}, {0.f, 0.f, 0.f, 0.f}, {0.f, 0.f, 0.f, 0.f}, {0.f, 0.f, 0.f, 0.f}};
    {
      v4u hg[4];
      const int hoff = WS_HP + g * 4096 + (14 * rgl + 4 * h) * 4;
      for (unsigned sp = 0; sp < SPIN_CAP; ++sp) {
        asm volatile("" ::: "memory");
#pragma unroll
        for (int r = 0; r < 4; ++r) hg[r] = __builtin_amdgcn_raw_buffer_load_b128(rw, hoff, r * 1024, AUX_SC1);
        const unsigned m01 = max(max3u(hg[0].x, hg[1].x, hg[2].x), max3u(hg[3].x, hg[0].y, hg[1].y)), m01b = max3u(m01, hg[2].y, hg[3].y);
        const unsigned m23 = max(max3u(hg[0].z, hg[1].z, hg[2].z), max3u(hg[3].z, hg[0].w, hg[1].w)), m23b = max3u(m23, hg[2].w, hg[3].w);
        const bool bad = (m01b == ~0u) || (h < 3 && m23b == ~0u);
        if (__builtin_amdgcn_ballot_w64(bad) == 0ull) break;
      }
      float hv[4];
#pragma unroll
      for (int j = 0; j < 4; ++j) {
        const float t = rbf((asf(hg[0][j]) + asf(hg[1][j])) + (asf(hg[2][j]) + asf(hg[3][j])));
        hv[j] = t * __builtin_amdgcn_rcpf(1.f + __builtin_amdgcn_exp2f(-1.4426950408889634f * t));
      }
      const v2u hf = {pk(hv[0], hv[1]), h < 3 ? pk(hv[2], hv[3]) : 0u};
#pragma unroll
      for (int ga = 0; ga < 4; ++ga) {
        const bool on = (c >> 2) == ga;
        const v2u hm = {on ? hf.x : 0u, on ? hf.y : 0u};
        const int cb = ga >> 1;
#pragma unroll
        for (int e2 = 0; e2 < 2; ++e2) {
          const int t = 2 * (ga & 1) + e2;
          acc[2 * e2] = mfma16(hm, v2u{perm_lo(fr[cb][0][t], fr[cb][1][t]), perm_lo(fr[cb][2][t], fr[cb][3][t])}, acc[2 * e2]);
          acc[2 * e2 + 1] = mfma16(hm, v2u{perm_hi(fr[cb][0][t], fr[cb][1][t]), perm_hi(fr[cb][2][t], fr[cb][3][t])}, acc[2 * e2 + 1]);
        }
      }
    }
    v4u wt[2][8];
#pragma unroll
    for (int cb = 0; cb < 2; ++cb)
#pragma unroll
      for (int t = 0; t < 4; ++t) {
        wt[cb][2 * t] = v4u{perm_lo(wr[cb][0][t], wr[cb][1][t]), perm_lo(wr[cb][2][t], wr[cb][3][t]), perm_lo(wr[cb][4][t], wr[cb][5][t]), perm_lo(wr[cb][6][t], wr[cb][7][t])};
        wt[cb][2 * t + 1] = v4u{perm_hi(wr[cb][0][t], wr[cb][1][t]), perm_hi(wr[cb][2][t], wr[cb][3][t]), perm_hi(wr[cb][4][t], wr[cb][5][t]), perm_hi(wr[cb][6][t], wr[cb][7][t])};
      }
#pragma unroll
    for (int cb = 0; cb < 2; ++cb)
#pragma unroll
      for (int t = 0; t < 8; ++t) asm volatile("" : "+v"(wt[cb][t]));
    if (b < 32) {
      const int coff = WS_QG + (((l >> 4) * 2 + (b >> 4)) * 512 + 32 * (l & 15)) * 8;
      for (unsigned sp = 0; sp < SPIN_CAP; ++sp) {
        asm volatile("" ::: "memory");
        const v2u t0 = __builtin_amdgcn_raw_buffer_load_b64(rw, coff, 0, AUX_SC1), t1 = __builtin_amdgcn_raw_buffer_load_b64(rw, coff, 4 * 8192, AUX_SC1);
        const unsigned bad = (t0.y ^ tag_hi) | (t1.y ^ tag_hi) | (((t0.x ^ tag_lo) | (t1.x ^ tag_lo)) & 0xffu);
        if (__builtin_amdgcn_ballot_w64(bad != 0u) == 0ull) break;
      }
    }
    STAMP(6);
    v4u pg[4];
    const int poff = WS_P2 + (rg * 32 + ((l >> 1) & 7)) * 128 + (2 * h + (l & 1)) * 16;
    for (unsigned sp = 0; sp < SPIN_CAP; ++sp) {
        asm volatile("" ::: "memory");
#pragma unroll
      for (int i = 0; i < 4; ++i) pg[i] = __builtin_amdgcn_raw_buffer_load_b128(rw, poff, i * 1024, AUX_SC1);
      const unsigned mx = max(max(max4(pg[0]), max4(pg[1])), max(max4(pg[2]), max4(pg[3])));
      if (__builtin_amdgcn_ballot_w64(mx == ~0u) == 0ull) break;
    }
    STAMP(7);
    float lse[4];
#pragma unroll
    for (int i = 0; i < 4; ++i)
      lse[i] = asf(__builtin_amdgcn_perm(__builtin_amdgcn_perm(pg[i].w, pg[i].z, 0x0c0c0400u), __builtin_amdgcn_perm(pg[i].y, pg[i].x, 0x0c0c0400u), 0x05040100u));
    float M = fmaxf(fmaxf(lse[0], lse[1]), fmaxf(lse[2], lse[3]));
    M = fmaxf(M, dpp<ROR8>(M)); M = fmaxf(M, dpp<ROR4>(M)); M = fmaxf(M, dpp<ROR2>(M));
    float ov[5] = {0.f, 0.f, 0.f, 0.f, 0.f};
#pragma unroll
    for (int i = 0; i < 4; ++i) {
      const float w = __builtin_amdgcn_exp2f(lse[i] - M);
      ov[4] += w;
#pragma unroll
      for (int e = 0; e < 4; ++e) ov[e] = fmaf(w, asf(pg[i][e]), ov[e]);
    }
#pragma unroll
    for (int e = 0; e < 5; ++e) { ov[e] += dpp<ROR8>(ov[e]); ov[e] += dpp<ROR4>(ov[e]); ov[e] += dpp<ROR2>(ov[e]); }
    const float inv = __builtin_amdgcn_rcpf(ov[4]);
    const unsigned oa = pk(ov[0] * inv, ov[1] * inv), ob = pk(ov[2] * inv, ov[3] * inv);
    const v4u of = {oa, ob, dppu<XOR1>(oa), dppu<XOR1>(ob)};
#pragma unroll
    for (int ga = 0; ga < 4; ++ga) {
      const bool on = (c >> 2) == ga;
      const v4u om = {on ? of.x : 0u, on ? of.y : 0u, on ? of.z : 0u, on ? of.w : 0u};
#pragma unroll
      for (int e = 0; e < 4; ++e) acc[e] = mfma32(om, wt[ga >> 1][4 * (ga & 1) + e], acc[e]);
    }
    const v4u p3 = {canon(asu(acc[0].x)), canon(asu(acc[1].x)), canon(asu(acc[2].x)), canon(asu(acc[3].x))};
    __builtin_amdgcn_raw_buffer_store_b128(p3, rw, WS_P3 + (ch * 128 + rg) * 1024 + l * 16, 0, AUX_SC1);
    STAMP(8);
    if (b < 32) {
      v4u fg[8];
      const int foff = WS_P3 + ((b >> 4) * 128 + (l >> 2)) * 1024 + rpos * 16;
      for (unsigned sp = 0; sp < SPIN_CAP; ++sp) {
        asm volatile("" ::: "memory");
#pragma unroll
        for (int i = 0; i < 8; ++i) fg[i] = __builtin_amdgcn_raw_buffer_load_b128(rw, foff, i * 16384, AUX_SC1);
        unsigned mx = 0;
#pragma unroll
        for (int i = 0; i < 8; ++i) mx = max(mx, max4(fg[i]));
        if (__builtin_amdgcn_ballot_w64(mx == ~0u) == 0ull) break;
      }
      STAMP(9);
      float t[4];
#pragma unroll
      for (int e = 0; e < 4; ++e)
        t[e] = ((asf(fg[0][e]) + asf(fg[1][e])) + (asf(fg[2][e]) + asf(fg[3][e]))) + ((asf(fg[4][e]) + asf(fg[5][e])) + (asf(fg[6][e]) + asf(fg[7][e])));
      float r = sw16add(sw32add(t[0], t[2]), sw32add(t[1], t[3]));
      r += dpp<ROR8>(r); r += dpp<ROR4>(r);
      const unsigned res = pk(lo16(xres) + r, 0.f);
      if ((l & 12) == 0) ((GAS u16*)out)[rcol] = (u16)res;
      if (b == 0 && l == 0) __hip_atomic_store((GAS u64*)ws, tag, __ATOMIC_RELAXED, __HIP_MEMORY_SCOPE_AGENT);
      STAMP(10);
    }
  }
  STAMP_OUT();
}

extern "C" void kernel_launch(void* const* d_in, const int* in_sizes, int n_in, void* d_out, int out_size, void* d_ws, size_t ws_size, hipStream_t stream) {
  fused_decode<<<256, 320, 0, stream>>>((const u16*)d_in[0], (const u16*)d_in[1], (const u16*)d_in[2], (const u16*)d_in[3], (const u16*)d_in[4],
                                        (const u16*)d_in[6], (const u16*)d_in[7], (u16*)d_out, (unsigned char*)d_ws);
}
```

```cpp
#define PROBE 0
#define PCLS 2
#define PBLK 0
#include <hip/hip_runtime.h>
#include <stdint.h>

#define DELTA 8
#ifndef NV_EARLY
#define NV_EARLY 0
#endif
#define CDELTA (DELTA + 2)

typedef unsigned short u16;
typedef unsigned long long u64;
typedef unsigned v4u __attribute__((ext_vector_type(4)));
typedef unsigned v2u __attribute__((ext_vector_type(2)));
typedef float f32x4 __attribute__((ext_vector_type(4)));
typedef float f32x2 __attribute__((ext_vector_type(2)));
typedef __bf16 bf16x2 __attribute__((ext_vector_type(2)));
typedef __bf16 bf16x8 __attribute__((ext_vector_type(8)));
typedef short s16x4 __attribute__((ext_vector_type(4)));
#define DI __device__ __forceinline__
#define GAS __attribute__((address_space(1)))
#define LAS __attribute__((address_space(3)))

constexpr int SK = 65536, WIN = 4096;
constexpr int AUX_NT = 2, AUX_SC1 = 16;
constexpr int WS_QG = 4096, WS_HP = WS_QG + 65536, WS_P2 = WS_HP + 32768, WS_P3 = WS_P2 + 524288, WS_END = WS_P3 + 262144;
constexpr unsigned SPIN_CAP = 1u << 18;
constexpr int L_QP = 0, L_HP = 512, L_QS = 1024, L_ST = L_QS + 4 * 272, L_TAG = L_ST + 128, L_ML = 2304, L_END = L_ML + 8192;

DI float asf(unsigned u) { return __builtin_bit_cast(float, u); }
DI unsigned asu(float f) { return __builtin_bit_cast(unsigned, f); }
DI unsigned pk(float lo, float hi) { f32x2 v = {lo, hi}; return __builtin_bit_cast(unsigned, __builtin_convertvector(v, bf16x2)); }
DI float lo16(unsigned u) { return asf(u << 16); }
DI float hi16(unsigned u) { return asf(u & 0xffff0000u); }
DI float rbf(float f) { return lo16(pk(f, f)); }
DI unsigned perm_lo(unsigned a, unsigned b) { return __builtin_amdgcn_perm(b, a, 0x05040100u); }
DI unsigned perm_hi(unsigned a, unsigned b) { return __builtin_amdgcn_perm(b, a, 0x07060302u); }
DI float dot2(unsigned a, unsigned b, float c) { return __builtin_amdgcn_fdot2_f32_bf16(__builtin_bit_cast(bf16x2, a), __builtin_bit_cast(bf16x2, b), c, false); }
template <int CTRL> DI float dpp(float x) { return asf(__builtin_amdgcn_mov_dpp(asu(x), CTRL, 0xf, 0xf, true)); }
template <int CTRL> DI unsigned dppu(unsigned x) { return __builtin_amdgcn_mov_dpp(x, CTRL, 0xf, 0xf, true); }
constexpr int ROR1 = 0x121, ROR2 = 0x122, ROR4 = 0x124, ROR8 = 0x128, XOR1 = 0xB1;
DI float sw32add(float a, float b) { auto r = __builtin_amdgcn_permlane32_swap(asu(a), asu(b), false, false); return asf(r[0]) + asf(r[1]); }
DI float sw16add(float a, float b) { auto r = __builtin_amdgcn_permlane16_swap(asu(a), asu(b), false, false); return asf(r[0]) + asf(r[1]); }
DI float sw32max(float a) { auto r = __builtin_amdgcn_permlane32_swap(asu(a), asu(a), false, false); return fmaxf(asf(r[0]), asf(r[1])); }
DI float sw16max(float a) { auto r = __builtin_amdgcn_permlane16_swap(asu(a), asu(a), false, false); return fmaxf(asf(r[0]), asf(r[1])); }
DI float wave_sum(float x) { x += dpp<ROR8>(x); x += dpp<ROR4>(x); x += dpp<ROR2>(x); x += dpp<ROR1>(x); x = sw16add(x, x); return sw32add(x, x); }
DI f32x4 mfma32(v4u a, v4u b, f32x4 c) { return __builtin_amdgcn_mfma_f32_16x16x32_bf16(__builtin_bit_cast(bf16x8, a), __builtin_bit_cast(bf16x8, b), c, 0, 0, 0); }
DI f32x4 mfma16(v2u a, v2u b, f32x4 c) { return __builtin_amdgcn_mfma_f32_16x16x16bf16_1k(__builtin_bit_cast(s16x4, a), __builtin_bit_cast(s16x4, b), c, 0, 0, 0); }
DI __amdgpu_buffer_rsrc_t rsrc(const void* p, int bytes) { return __builtin_amdgcn_make_buffer_rsrc((void*)p, 0, bytes, 0x00020000); }
DI void barrier() { asm volatile("s_waitcnt lgkmcnt(0)\n\ts_barrier" ::: "memory"); }
DI void fence_sched() { __builtin_amdgcn_sched_barrier(0); }
DI unsigned max3u(unsigned a, unsigned b, unsigned c) { return max(max(a, b), c); }
DI unsigned max4(v4u v) { return max(max(v.x, v.y), max(v.z, v.w)); }
DI unsigned canon(unsigned v) { return min(v, 0xfffffffeu); }

DI u64 rt() { u64 t; fence_sched(); asm volatile("s_memrealtime %0\n\ts_waitcnt lgkmcnt(0)" : "=s"(t) :: "memory"); fence_sched(); return t; }
#define STAMP(i) do { if ((i) == PROBE) st_ = rt(); } while (0)
#define STAMP_DECL u64 st_ = 0
#define STAMP_OUT() do { if (st_ != 0 && (PCLS == 2 || (wave == 0) == (PCLS == 0)) && (PBLK < 0 || b == PBLK)) { while (rt() < st_ + 3000) __builtin_amdgcn_s_sleep(1); } } while (0)

DI f32x2 reduce8(const float (&a)[8]) {
  float t0 = sw32add(a[0], a[4]), t1 = sw32add(a[1], a[5]), t2 = sw32add(a[2], a[6]), t3 = sw32add(a[3], a[7]);
  float u0 = sw16add(t0, t2), u1 = sw16add(t1, t3);
  u0 += dpp<ROR8>(u0); u1 += dpp<ROR8>(u1); u0 += dpp<ROR4>(u0); u1 += dpp<ROR4>(u1);
  return f32x2{u0, u1};
}

__global__ void __launch_bounds__(320, 2)
fused_decode(const u16* __restrict__ x, const u16* __restrict__ Kc, const u16* __restrict__ Vc, const u16* __restrict__ wq, const u16* __restrict__ wo,
             const u16* __restrict__ w1, const u16* __restrict__ w2, u16* __restrict__ out, unsigned char* ws) {
  __shared__ __attribute__((aligned(16))) unsigned char lds_[L_END];
  LAS unsigned char* L = (LAS unsigned char*)lds_;
  STAMP_DECL;
  const int b = blockIdx.x, g = b & 7, s = b >> 3;
  const int l = threadIdx.x & 63, wave = __builtin_amdgcn_readfirstlane(threadIdx.x >> 6);
  const int c = l & 15, h = l >> 4;
  STAMP(0);
  const __amdgpu_buffer_rsrc_t rw = rsrc(ws, WS_END);
  asm volatile("" :: "s"(x), "s"(Kc), "s"(Vc), "s"(wq), "s"(wo), "s"(w1), "s"(w2), "s"(out), "s"(ws));

  if (wave != 0) {
    const int a = wave - 1, u = l >> 2, cq = l & 3;
    const __amdgpu_buffer_rsrc_t rx = rsrc(x, 1024), rq = rsrc(wq, 512 * 4096 * 2), r1 = rsrc(w1, s < 28 ? 512 * 1792 * 2 : 0);
    const size_t kvoff = ((size_t)g * SK + (SK - WIN) + s * 128 + a * 32) * 128;
    const __amdgpu_buffer_rsrc_t rk = rsrc(Kc + kvoff, 8192), rv = rsrc(Vc + kvoff, 8192);
    const int R0 = 256 * (s & 1) + 64 * a + 4 * u, R1 = 128 * (s & 3) + 32 * a + 2 * u;
    const v4u xf = __builtin_amdgcn_raw_buffer_load_b128(rx, l * 16, 0, 0);
    const v2u x4 = __builtin_amdgcn_raw_buffer_load_b64(rx, R0 * 2, 0, 0);
    const unsigned x2 = __builtin_amdgcn_raw_buffer_load_b32(rx, R1 * 2, 0, 0);
    v4u wqr[4], w1r[2], kr[8], vr[8];
    const int qoff = (R0 * 4096 + 512 * g + 32 * (s >> 1) + 8 * cq) * 2;
#pragma unroll
    for (int i = 0; i < 4; ++i) wqr[i] = __builtin_amdgcn_raw_buffer_load_b128(rq, qoff, i * 8192, AUX_NT);
    const int off1 = (R1 * 1792 + 224 * g + 32 * (s >> 2) + 8 * cq) * 2;
#pragma unroll
    for (int i = 0; i < 2; ++i) w1r[i] = __builtin_amdgcn_raw_buffer_load_b128(r1, off1, i * 3584, AUX_NT);
#pragma unroll
    for (int i = 0; i < 8; ++i) kr[i] = __builtin_amdgcn_raw_buffer_load_b128(rk, c * 256 + h * 16 + (i & 3) * 64, (i >> 2) * 4096, AUX_NT);
#pragma unroll
    for (int j = 0; j < NV_EARLY; ++j) vr[j] = __builtin_amdgcn_raw_buffer_load_b128(rv, h * 1024 + c * 16 + (j & 3) * 256, (j >> 2) * 4096, AUX_NT);
    fence_sched();
    STAMP(1);
    float xv[8];
#pragma unroll
    for (int i = 0; i < 4; ++i) { xv[2 * i] = lo16(xf[i]); xv[2 * i + 1] = hi16(xf[i]); }
    float sm = ((xv[0] + xv[1]) + (xv[2] + xv[3])) + ((xv[4] + xv[5]) + (xv[6] + xv[7]));
    const float mean = wave_sum(sm) * (1.f / 512.f);
    float sq = 0.f;
#pragma unroll
    for (int i = 0; i < 8; ++i) { const float d = xv[i] - mean; sq = fmaf(d, d, sq); }
    const float var = wave_sum(sq) * (1.f / 512.f);
    const float mb = rbf(mean);
    const float rden = __builtin_amdgcn_rcpf(rbf(__builtin_amdgcn_sqrtf(rbf(rbf(var) + 1e-5f))));
    auto norm2 = [&](unsigned p) { const unsigned d = pk(lo16(p) - mb, hi16(p) - mb); return pk(lo16(d) * rden, hi16(d) * rden); };
    const unsigned xq0 = norm2(x4.x), xq1 = norm2(x4.y), xh = norm2(x2);
    {
      float acc[8];
#pragma unroll
      for (int t = 0; t < 4; ++t) {
        acc[2 * t] = dot2(perm_lo(wqr[2][t], wqr[3][t]), xq1, dot2(perm_lo(wqr[0][t], wqr[1][t]), xq0, 0.f));
        acc[2 * t + 1] = dot2(perm_hi(wqr[2][t], wqr[3][t]), xq1, dot2(perm_hi(wqr[0][t], wqr[1][t]), xq0, 0.f));
      }
      const f32x2 r = reduce8(acc);
      if ((l & 12) == 0) *(LAS f32x2*)(L + L_QP + a * 128 + (8 * cq + 2 * h) * 4) = r;
    }
    STAMP(2);
    barrier();
    {
      float acc[8];
#pragma unroll
      for (int t = 0; t < 4; ++t) {
        acc[2 * t] = dot2(perm_lo(w1r[0][t], w1r[1][t]), xh, 0.f);
        acc[2 * t + 1] = dot2(perm_hi(w1r[0][t], w1r[1][t]), xh, 0.f);
      }
      const f32x2 r = reduce8(acc);
      if ((l & 12) == 0) *(LAS f32x2*)(L + L_HP + a * 128 + (8 * cq + 2 * h) * 4) = r;
    }
    barrier();
    if (DELTA) __builtin_amdgcn_s_sleep(DELTA);
    STAMP(3);
    const v2u tg = *(const LAS v2u*)(L + L_TAG);
    const unsigned tag_lo = tg.x, tag_hi = tg.y;
    const int goff = WS_QG + (g * 1024 + 128 * a + 2 * l) * 8;
    v4u g0 = __builtin_amdgcn_raw_buffer_load_b128(rw, goff, 0, AUX_SC1);
    v4u g1 = __builtin_amdgcn_raw_buffer_load_b128(rw, goff, 4096, AUX_SC1);
#pragma unroll
    for (int j = NV_EARLY; j < 8; ++j) vr[j] = __builtin_amdgcn_raw_buffer_load_b128(rv, h * 1024 + c * 16 + (j & 3) * 256, (j >> 2) * 4096, AUX_NT);
    fence_sched();
    auto qbad = [&](v4u p, v4u q) {
      const unsigned hi = (p.y ^ tag_hi) | (p.w ^ tag_hi) | (q.y ^ tag_hi) | (q.w ^ tag_hi);
      const unsigned lo = ((p.x ^ tag_lo) | (p.z ^ tag_lo) | (q.x ^ tag_lo) | (q.z ^ tag_lo)) & 0xffu;
      return __builtin_amdgcn_ballot_w64((hi | lo) != 0u) != 0ull; };
    if (__builtin_expect(qbad(g0, g1), 0)) {
      STAMP(9);
      for (unsigned sp = 0; sp < SPIN_CAP; ++sp) {
        asm volatile("" ::: "memory");
        g0 = __builtin_amdgcn_raw_buffer_load_b128(rw, goff, 0, AUX_SC1);
        g1 = __builtin_amdgcn_raw_buffer_load_b128(rw, goff, 4096, AUX_SC1);
        if (!qbad(g0, g1)) break;
      }
    }
    *(LAS unsigned*)(L + L_QS + a * 272 + l * 4) = pk(asf(g0.x & 0xffffff00u) + asf(g1.x & 0xffffff00u), asf(g0.z & 0xffffff00u) + asf(g1.z & 0xffffff00u));
    STAMP(4);
    barrier();
    f32x4 sc[2] = {{0.f, 0.f, 0.f, 0.f}, {0.f, 0.f, 0.f, 0.f}};
#pragma unroll
    for (int t = 0; t < 4; ++t) {
      const v4u qf = *(const LAS v4u*)(L + L_QS + (c & 3) * 272 + t * 64 + h * 16);
      sc[0] = mfma32(kr[t], qf, sc[0]);
      sc[1] = mfma32(kr[4 + t], qf, sc[1]);
    }
    float sv[8];
#pragma unroll
    for (int i = 0; i < 4; ++i) {
      const unsigned r1_ = pk(sc[i >> 1][2 * (i & 1)], sc[i >> 1][2 * (i & 1) + 1]);
      const unsigned r2_ = pk(lo16(r1_) * 0.08838834764831845f, hi16(r1_) * 0.08838834764831845f);
      sv[2 * i] = lo16(r2_); sv[2 * i + 1] = hi16(r2_);
    }
    float m = fmaxf(fmaxf(fmaxf(sv[0], sv[1]), fmaxf(sv[2], sv[3])), fmaxf(fmaxf(sv[4], sv[5]), fmaxf(sv[6], sv[7])));
    m = sw32max(sw16max(m));
    constexpr float LOG2E = 1.4426950408889634f;
    float p[8], ls = 0.f;
#pragma unroll
    for (int i = 0; i < 8; ++i) { p[i] = __builtin_amdgcn_exp2f((sv[i] - m) * LOG2E); ls += p[i]; }
    ls = sw16add(ls, ls); ls = sw32add(ls, ls);
    if (l < 4) *(LAS f32x2*)(L + L_ST + l * 32 + a * 8) = f32x2{m * LOG2E, ls};
    const v4u pf = {pk(p[0], p[1]), pk(p[2], p[3]), pk(p[4], p[5]), pk(p[6], p[7])};
    STAMP(5);
    f32x4 oa[2] = {{0.f, 0.f, 0.f, 0.f}, {0.f, 0.f, 0.f, 0.f}};
#pragma unroll
    for (int ga = 0; ga < 4; ++ga) {
      const bool on = (c >> 2) == ga;
      const v4u pm = {on ? pf.x : 0u, on ? pf.y : 0u, on ? pf.z : 0u, on ? pf.w : 0u};
      const v4u v0 = {perm_lo(vr[0][ga], vr[1][ga]), perm_lo(vr[2][ga], vr[3][ga]), perm_lo(vr[4][ga], vr[5][ga]), perm_lo(vr[6][ga], vr[7][ga])};
      const v4u v1 = {perm_hi(vr[0][ga], vr[1][ga]), perm_hi(vr[2][ga], vr[3][ga]), perm_hi(vr[4][ga], vr[5][ga]), perm_hi(vr[6][ga], vr[7][ga])};
      oa[0] = mfma32(v0, pm, oa[0]);
      oa[1] = mfma32(v1, pm, oa[1]);
    }
    *(LAS f32x4*)(L + L_ML + a * 2048 + l * 16) = oa[0];
    *(LAS f32x4*)(L + L_ML + a * 2048 + 1024 + l * 16) = oa[1];
    STAMP(6);
    barrier();
    {
      const int qd = (l >> 3) & 3, kap = l & 7;
      const f32x4 s01 = *(const LAS f32x4*)(L + L_ST + a * 32), s23 = *(const LAS f32x4*)(L + L_ST + a * 32 + 16);
      const int src = L_ML + (kap & 1) * 1024 + (4 * (kap >> 1) + a + 16 * qd) * 16;
      const f32x4 o0 = *(const LAS f32x4*)(L + src), o1 = *(const LAS f32x4*)(L + src + 2048), o2 = *(const LAS f32x4*)(L + src + 4096), o3 = *(const LAS f32x4*)(L + src + 6144);
      const float M = fmaxf(fmaxf(s01.x, s01.z), fmaxf(s23.x, s23.z));
      float w0 = __builtin_amdgcn_exp2f(s01.x - M), w1_ = __builtin_amdgcn_exp2f(s01.z - M), w2_ = __builtin_amdgcn_exp2f(s23.x - M), w3 = __builtin_amdgcn_exp2f(s23.z - M);
      const float Ls = (w0 * s01.y + w1_ * s01.w) + (w2_ * s23.y + w3 * s23.w);
      const float inv = __builtin_amdgcn_rcpf(Ls);
      const unsigned lse = asu(M + __builtin_amdgcn_logf(Ls));
      w0 *= inv; w1_ *= inv; w2_ *= inv; w3 *= inv;
      const f32x4 o = (o0 * w0 + o1 * w1_) + (o2 * w2_ + o3 * w3);
      const v4u pv = {canon(__builtin_amdgcn_perm(asu(o.x), lse, 0x07060500u)), canon(__builtin_amdgcn_perm(asu(o.y), lse, 0x07060501u)),
                      canon(__builtin_amdgcn_perm(asu(o.z), lse, 0x07060502u)), canon(__builtin_amdgcn_perm(asu(o.w), lse, 0x07060503u))};
      if (l < 32) __builtin_amdgcn_raw_buffer_store_b128(pv, rw, WS_P2 + (((4 * g + a) * 4 + qd) * 32 + s) * 128 + kap * 16, 0, AUX_SC1);
    }
    STAMP(7);
  } else {
    const int rgl = s >> 1, ch = s & 1, rg = 16 * g + rgl;
    const v2u ep = __builtin_amdgcn_raw_buffer_load_b64(rw, 0, 0, 0);
    const v4u ones = {~0u, ~0u, ~0u, ~0u};
    __builtin_amdgcn_raw_buffer_store_b128(ones, rw, WS_P2 + ((16 * g + (l >> 3)) * 32 + s) * 128 + (l & 7) * 16, 0, AUX_SC1);
    __builtin_amdgcn_raw_buffer_store_b128(ones, rw, WS_P2 + ((16 * g + 8 + (l >> 3)) * 32 + s) * 128 + (l & 7) * 16, 0, AUX_SC1);
    __builtin_amdgcn_raw_buffer_store_b128(ones, rw, WS_P3 + (ch * 128 + rg) * 1024 + l * 16, 0, AUX_SC1);
    const int hpoff = WS_HP + (g * 4 + (s & 3)) * 1024 + (s >> 2) * 128;
    if (l < 8) __builtin_amdgcn_raw_buffer_store_b128(ones, rw, hpoff + l * 16, 0, AUX_SC1);
    asm volatile("s_waitcnt vmcnt(0)" ::: "memory");
    const u64 tag = (((u64)ep.y << 32) | ep.x) + 1;
    const unsigned tag_lo = (unsigned)tag & 0xffu, tag_hi = (unsigned)(tag >> 8);
    *(LAS v2u*)(L + L_TAG) = v2u{tag_lo, tag_hi};
    STAMP(1);
    barrier();
    {
      const LAS float* qp = (const LAS float*)(L + L_QP) + (l & 31);
      const float q = (qp[0] + qp[32]) + (qp[64] + qp[96]);
      const v2u gr = {(asu(q) & 0xffffff00u) | tag_lo, tag_hi};
      if (l < 32) __builtin_amdgcn_raw_buffer_store_b64(gr, rw, WS_QG + ((g * 2 + (s & 1)) * 512 + 32 * (s >> 1) + l) * 8, 0, AUX_SC1);
    }
    STAMP(2);
    barrier();
    {
      const LAS float* hp = (const LAS float*)(L + L_HP) + (l & 31);
      const float hv = (hp[0] + hp[32]) + (hp[64] + hp[96]);
      if (l < 32) __builtin_amdgcn_raw_buffer_store_b32(canon(asu(hv)), rw, hpoff + l * 4, 0, AUX_SC1);
    }
    if (CDELTA) __builtin_amdgcn_s_sleep(CDELTA);
    STAMP(3);
    const __amdgpu_buffer_rsrc_t ro = rsrc(wo, 4096 * 512 * 2), r2 = rsrc(w2, 1792 * 512 * 2), rx = rsrc(x, 1024);
    v4u wr[2][8], fr[2][4];
    const int ooff = ((32 * rg + 2 * h) * 512 + 256 * ch + 8 * c) * 2;
#pragma unroll
    for (int j = 0; j < 8; ++j)
#pragma unroll
      for (int cb = 0; cb < 2; ++cb) wr[cb][j] = __builtin_amdgcn_raw_buffer_load_b128(ro, ooff + cb * 256, (8 * (j & 3) + (j >> 2)) * 1024, AUX_NT);
#pragma unroll
    for (int j = 0; j < 4; ++j)
#pragma unroll
      for (int cb = 0; cb < 2; ++cb) fr[cb][j] = __builtin_amdgcn_raw_buffer_load_b128(r2, ((14 * rg + min(4 * h + j, 13)) * 512 + 256 * ch + 8 * c) * 2 + cb * 256, 0, AUX_NT);
    const int rpos = 4 * (b & 15) + (l & 3);
    const int rcol = 256 * ((b >> 4) & 1) + 128 * (rpos >> 5) + 8 * (rpos & 15) + 4 * ((rpos >> 4) & 1) + h;
    const unsigned xres = __builtin_amdgcn_raw_buffer_load_b16(rx, rcol * 2, 0, 0);
    fence_sched();
    STAMP(4);
    barrier();
    barrier();
    STAMP(5);
    f32x4 acc[4] = {{0.f, 0.f, 0.f, 0.f}, {0.f, 0.f, 0.f, 0.f}, {0.f, 0.f, 0.f, 0.f}, {0.f, 0.f, 0.f, 0.f}};
    {
      v4u hg[4];
      const int hoff = WS_HP + g * 4096 + (14 * rgl + 4 * h) * 4;
      for (unsigned sp = 0; sp < SPIN_CAP; ++sp) {
        asm volatile("" ::: "memory");
#pragma unroll
        for (int r = 0; r < 4; ++r) hg[r] = __builtin_amdgcn_raw_buffer_load_b128(rw, hoff, r * 1024, AUX_SC1);
        const unsigned m01 = max(max3u(hg[0].x, hg[1].x, hg[2].x), max3u(hg[3].x, hg[0].y, hg[1].y)), m01b = max3u(m01, hg[2].y, hg[3].y);
        const unsigned m23 = max(max3u(hg[0].z, hg[1].z, hg[2].z), max3u(hg[3].z, hg[0].w, hg[1].w)), m23b = max3u(m23, hg[2].w, hg[3].w);
        const bool bad = (m01b == ~0u) || (h < 3 && m23b == ~0u);
        if (__builtin_amdgcn_ballot_w64(bad) == 0ull) break;
      }
      float hv[4];
#pragma unroll
      for (int j = 0; j < 4; ++j) {
        const float t = rbf((asf(hg[0][j]) + asf(hg[1][j])) + (asf(hg[2][j]) + asf(hg[3][j])));
        hv[j] = t * __builtin_amdgcn_rcpf(1.f + __builtin_amdgcn_exp2f(-1.4426950408889634f * t));
      }
      const v2u hf = {pk(hv[0], hv[1]), h < 3 ? pk(hv[2], hv[3]) : 0u};
#pragma unroll
      for (int ga = 0; ga < 4; ++ga) {
        const bool on = (c >> 2) == ga;
        const v2u hm = {on ? hf.x : 0u, on ? hf.y : 0u};
        const int cb = ga >> 1;
#pragma unroll
        for (int e2 = 0; e2 < 2; ++e2) {
          const int t = 2 * (ga & 1) + e2;
          acc[2 * e2] = mfma16(hm, v2u{perm_lo(fr[cb][0][t], fr[cb][1][t]), perm_lo(fr[cb][2][t], fr[cb][3][t])}, acc[2 * e2]);
          acc[2 * e2 + 1] = mfma16(hm, v2u{perm_hi(fr[cb][0][t], fr[cb][1][t]), perm_hi(fr[cb][2][t], fr[cb][3][t])}, acc[2 * e2 + 1]);
        }
      }
    }
    v4u wt[2][8];
#pragma unroll
    for (int cb = 0; cb < 2; ++cb)
#pragma unroll
      for (int t = 0; t < 4; ++t) {
        wt[cb][2 * t] = v4u{perm_lo(wr[cb][0][t], wr[cb][1][t]), perm_lo(wr[cb][2][t], wr[cb][3][t]), perm_lo(wr[cb][4][t], wr[cb][5][t]), perm_lo(wr[cb][6][t], wr[cb][7][t])};
        wt[cb][2 * t + 1] = v4u{perm_hi(wr[cb][0][t], wr[cb][1][t]), perm_hi(wr[cb][2][t], wr[cb][3][t]), perm_hi(wr[cb][4][t], wr[cb][5][t]), perm_hi(wr[cb][6][t], wr[cb][7][t])};
      }
#pragma unroll
    for (int cb = 0; cb < 2; ++cb)
#pragma unroll
      for (int t = 0; t < 8; ++t) asm volatile("" : "+v"(wt[cb][t]));
    if (b < 32) {
      const int coff = WS_QG + (((l >> 4) * 2 + (b >> 4)) * 512 + 32 * (l & 15)) * 8;
      for (unsigned sp = 0; sp < SPIN_CAP; ++sp) {
        asm volatile("" ::: "memory");
        const v2u t0 = __builtin_amdgcn_raw_buffer_load_b64(rw, coff, 0, AUX_SC1), t1 = __builtin_amdgcn_raw_buffer_load_b64(rw, coff, 4 * 8192, AUX_SC1);
        const unsigned bad = (t0.y ^ tag_hi) | (t1.y ^ tag_hi) | (((t0.x ^ tag_lo) | (t1.x ^ tag_lo)) & 0xffu);
        if (__builtin_amdgcn_ballot_w64(bad != 0u) == 0ull) break;
      }
    }
    STAMP(6);
    v4u pg[4];
    const int poff = WS_P2 + (rg * 32 + ((l >> 1) & 7)) * 128 + (2 * h + (l & 1)) * 16;
    for (unsigned sp = 0; sp < SPIN_CAP; ++sp) {
        asm volatile("" ::: "memory");
#pragma unroll
      for (int i = 0; i < 4; ++i) pg[i] = __builtin_amdgcn_raw_buffer_load_b128(rw, poff, i * 1024, AUX_SC1);
      const unsigned mx = max(max(max4(pg[0]), max4(pg[1])), max(max4(pg[2]), max4(pg[3])));
      if (__builtin_amdgcn_ballot_w64(mx == ~0u) == 0ull) break;
    }
    STAMP(7);
    float lse[4];
#pragma unroll
    for (int i = 0; i < 4; ++i)
      lse[i] = asf(__builtin_amdgcn_perm(__builtin_amdgcn_perm(pg[i].w, pg[i].z, 0x0c0c0400u), __builtin_amdgcn_perm(pg[i].y, pg[i].x, 0x0c0c0400u), 0x05040100u));
    float M = fmaxf(fmaxf(lse[0], lse[1]), fmaxf(lse[2], lse[3]));
    M = fmaxf(M, dpp<ROR8>(M)); M = fmaxf(M, dpp<ROR4>(M)); M = fmaxf(M, dpp<ROR2>(M));
    float ov[5] = {0.f, 0.f, 0.f, 0.f, 0.f};
#pragma unroll
    for (int i = 0; i < 4; ++i) {
      const float w = __builtin_amdgcn_exp2f(lse[i] - M);
      ov[4] += w;
#pragma unroll
      for (int e = 0; e < 4; ++e) ov[e] = fmaf(w, asf(pg[i][e]), ov[e]);
    }
#pragma unroll
    for (int e = 0; e < 5; ++e) { ov[e] += dpp<ROR8>(ov[e]); ov[e] += dpp<ROR4>(ov[e]); ov[e] += dpp<ROR2>(ov[e]); }
    const float inv = __builtin_amdgcn_rcpf(ov[4]);
    const unsigned oa = pk(ov[0] * inv, ov[1] * inv), ob = pk(ov[2] * inv, ov[3] * inv);
    const v4u of = {oa, ob, dppu<XOR1>(oa), dppu<XOR1>(ob)};
#pragma unroll
    for (int ga = 0; ga < 4; ++ga) {
      const bool on = (c >> 2) == ga;
      const v4u om = {on ? of.x : 0u, on ? of.y : 0u, on ? of.z : 0u, on ? of.w : 0u};
#pragma unroll
      for (int e = 0; e < 4; ++e) acc[e] = mfma32(om, wt[ga >> 1][4 * (ga & 1) + e], acc[e]);
    }
    const v4u p3 = {canon(asu(acc[0].x)), canon(asu(acc[1].x)), canon(asu(acc[2].x)), canon(asu(acc[3].x))};
    __builtin_amdgcn_raw_buffer_store_b128(p3, rw, WS_P3 + (ch * 128 + rg) * 1024 + l * 16, 0, AUX_SC1);
    STAMP(8);
    if (b < 32) {
      v4u fg[8];
      const int foff = WS_P3 + ((b >> 4) * 128 + (l >> 2)) * 1024 + rpos * 16;
      for (unsigned sp = 0; sp < SPIN_CAP; ++sp) {
        asm volatile("" ::: "memory");
#pragma unroll
        for (int i = 0; i < 8; ++i) fg[i] = __builtin_amdgcn_raw_buffer_load_b128(rw, foff, i * 16384, AUX_SC1);
        unsigned mx = 0;
#pragma unroll
        for (int i = 0; i < 8; ++i) mx = max(mx, max4(fg[i]));
        if (__builtin_amdgcn_ballot_w64(mx == ~0u) == 0ull) break;
      }
      STAMP(9);
      float t[4];
#pragma unroll
      for (int e = 0; e < 4; ++e)
        t[e] = ((asf(fg[0][e]) + asf(fg[1][e])) + (asf(fg[2][e]) + asf(fg[3][e]))) + ((asf(fg[4][e]) + asf(fg[5][e])) + (asf(fg[6][e]) + asf(fg[7][e])));
      float r = sw16add(sw32add(t[0], t[2]), sw32add(t[1], t[3]));
      r += dpp<ROR8>(r); r += dpp<ROR4>(r);
      const unsigned res = pk(lo16(xres) + r, 0.f);
      if ((l & 12) == 0) ((GAS u16*)out)[rcol] = (u16)res;
      if (b == 0 && l == 0) __hip_atomic_store((GAS u64*)ws, tag, __ATOMIC_RELAXED, __HIP_MEMORY_SCOPE_AGENT);
      STAMP(10);
    }
  }
  STAMP_OUT();
}

extern "C" void kernel_launch(void* const* d_in, const int* in_sizes, int n_in, void* d_out, int out_size, void* d_ws, size_t ws_size, hipStream_t stream) {
  fused_decode<<<256, 320, 0, stream>>>((const u16*)d_in[0], (const u16*)d_in[1], (const u16*)d_in[2], (const u16*)d_in[3], (const u16*)d_in[4],
                                        (const u16*)d_in[6], (const u16*)d_in[7], (u16*)d_out, (unsigned char*)d_ws);
}
```

```cpp
#define PROBE 99
#define PCLS 2
#define PBLK 0
#include <hip/hip_runtime.h>
#include <stdint.h>

#define DELTA 8
#ifndef NV_EARLY
#define NV_EARLY 0
#endif
#define CDELTA (DELTA + 2)

typedef unsigned short u16;
typedef unsigned long long u64;
typedef unsigned v4u __attribute__((ext_vector_type(4)));
typedef unsigned v2u __attribute__((ext_vector_type(2)));
typedef float f32x4 __attribute__((ext_vector_type(4)));
typedef float f32x2 __attribute__((ext_vector_type(2)));
typedef __bf16 bf16x2 __attribute__((ext_vector_type(2)));
typedef __bf16 bf16x8 __attribute__((ext_vector_type(8)));
typedef short s16x4 __attribute__((ext_vector_type(4)));
#define DI __device__ __forceinline__
#define GAS __attribute__((address_space(1)))
#define LAS __attribute__((address_space(3)))

constexpr int SK = 65536, WIN = 4096;
constexpr int AUX_NT = 2, AUX_SC1 = 16;
constexpr int WS_QG = 4096, WS_HP = WS_QG + 65536, WS_P2 = WS_HP + 32768, WS_P3 = WS_P2 + 524288, WS_END = WS_P3 + 262144;
constexpr unsigned SPIN_CAP = 1u << 18;
constexpr int L_QP = 0, L_HP = 512, L_QS = 1024, L_ST = L_QS + 4 * 272, L_TAG = L_ST + 128, L_ML = 2304, L_END = L_ML + 8192;

DI float asf(unsigned u) { return __builtin_bit_cast(float, u); }
DI unsigned asu(float f) { return __builtin_bit_cast(unsigned, f); }
DI unsigned pk(float lo, float hi) { f32x2 v = {lo, hi}; return __builtin_bit_cast(unsigned, __builtin_convertvector(v, bf16x2)); }
DI float lo16(unsigned u) { return asf(u << 16); }
DI float hi16(unsigned u) { return asf(u & 0xffff0000u); }
DI float rbf(float f) { return lo16(pk(f, f)); }
DI unsigned perm_lo(unsigned a, unsigned b) { return __builtin_amdgcn_perm(b, a, 0x05040100u); }
DI unsigned perm_hi(unsigned a, unsigned b) { return __builtin_amdgcn_perm(b, a, 0x07060302u); }
DI float dot2(unsigned a, unsigned b, float c) { return __builtin_amdgcn_fdot2_f32_bf16(__builtin_bit_cast(bf16x2, a), __builtin_bit_cast(bf16x2, b), c, false); }
template <int CTRL> DI float dpp(float x) { return asf(__builtin_amdgcn_mov_dpp(asu(x), CTRL, 0xf, 0xf, true)); }
template <int CTRL> DI unsigned dppu(unsigned x) { return __builtin_amdgcn_mov_dpp(x, CTRL, 0xf, 0xf, true); }
constexpr int ROR1 = 0x121, ROR2 = 0x122, ROR4 = 0x124, ROR8 = 0x128, XOR1 = 0xB1;
DI float sw32add(float a, float b) { auto r = __builtin_amdgcn_permlane32_swap(asu(a), asu(b), false, false); return asf(r[0]) + asf(r[1]); }
DI float sw16add(float a, float b) { auto r = __builtin_amdgcn_permlane16_swap(asu(a), asu(b), false, false); return asf(r[0]) + asf(r[1]); }
DI float sw32max(float a) { auto r = __builtin_amdgcn_permlane32_swap(asu(a), asu(a), false, false); return fmaxf(asf(r[0]), asf(r[1])); }
DI float sw16max(float a) { auto r = __builtin_amdgcn_permlane16_swap(asu(a), asu(a), false, false); return fmaxf(asf(r[0]), asf(r[1])); }
DI float wave_sum(float x) { x += dpp<ROR8>(x); x += dpp<ROR4>(x); x += dpp<ROR2>(x); x += dpp<ROR1>(x); x = sw16add(x, x); return sw32add(x, x); }
DI f32x4 mfma32(v4u a, v4u b, f32x4 c) { return __builtin_amdgcn_mfma_f32_16x16x32_bf16(__builtin_bit_cast(bf16x8, a), __builtin_bit_cast(bf16x8, b), c, 0, 0, 0); }
DI f32x4 mfma16(v2u a, v2u b, f32x4 c) { return __builtin_amdgcn_mfma_f32_16x16x16bf16_1k(__builtin_bit_cast(s16x4, a), __builtin_bit_cast(s16x4, b), c, 0, 0, 0); }
DI __amdgpu_buffer_rsrc_t rsrc(const void* p, int bytes) { return __builtin_amdgcn_make_buffer_rsrc((void*)p, 0, bytes, 0x00020000); }
DI void barrier() { asm volatile("s_waitcnt lgkmcnt(0)\n\ts_barrier" ::: "memory"); }
DI void fence_sched() { __builtin_amdgcn_sched_barrier(0); }
DI unsigned max3u(unsigned a, unsigned b, unsigned c) { return max(max(a, b), c); }
DI unsigned max4(v4u v) { return max(max(v.x, v.y), max(v.z, v.w)); }
DI unsigned canon(unsigned v) { return min(v, 0xfffffffeu); }

DI u64 rt() { u64 t; fence_sched(); asm volatile("s_memrealtime %0\n\ts_waitcnt lgkmcnt(0)" : "=s"(t) :: "memory"); fence_sched(); return t; }
#define STAMP(i) do { if ((i) == PROBE) st_ = rt(); } while (0)
#define STAMP_DECL u64 st_ = 0
#define STAMP_OUT() do { if (st_ != 0 && (PCLS == 2 || (wave == 0) == (PCLS == 0)) && (PBLK < 0 || b == PBLK)) { while (rt() < st_ + 3000) __builtin_amdgcn_s_sleep(1); } } while (0)

DI f32x2 reduce8(const float (&a)[8]) {
  float t0 = sw32add(a[0], a[4]), t1 = sw32add(a[1], a[5]), t2 = sw32add(a[2], a[6]), t3 = sw32add(a[3], a[7]);
  float u0 = sw16add(t0, t2), u1 = sw16add(t1, t3);
  u0 += dpp<ROR8>(u0); u1 += dpp<ROR8>(u1); u0 += dpp<ROR4>(u0); u1 += dpp<ROR4>(u1);
  return f32x2{u0, u1};
}

__global__ void __launch_bounds__(320, 2)
fused_decode(const u16* __restrict__ x, const u16* __restrict__ Kc, const u16* __restrict__ Vc, const u16* __restrict__ wq, const u16* __restrict__ wo,
             const u16* __restrict__ w1, const u16* __restrict__ w2, u16* __restrict__ out, unsigned char* ws) {
  __shared__ __attribute__((aligned(16))) unsigned char lds_[L_END];
  LAS unsigned char* L = (LAS unsigned char*)lds_;
  STAMP_DECL;
  const int b = blockIdx.x, g = b & 7, s = b >> 3;
  const int l = threadIdx.x & 63, wave = __builtin_amdgcn_readfirstlane(threadIdx.x >> 6);
  const int c = l & 15, h = l >> 4;
  STAMP(0);
  const __amdgpu_buffer_rsrc_t rw = rsrc(ws, WS_END);
  asm volatile("" :: "s"(x), "s"(Kc), "s"(Vc), "s"(wq), "s"(wo), "s"(w1), "s"(w2), "s"(out), "s"(ws));

  if (wave != 0) {
    const int a = wave - 1, u = l >> 2, cq = l & 3;
    const __amdgpu_buffer_rsrc_t rx = rsrc(x, 1024), rq = rsrc(wq, 512 * 4096 * 2), r1 = rsrc(w1, s < 28 ? 512 * 1792 * 2 : 0);
    const size_t kvoff = ((size_t)g * SK + (SK - WIN) + s * 128 + a * 32) * 128;
    const __amdgpu_buffer_rsrc_t rk = rsrc(Kc + kvoff, 8192), rv = rsrc(Vc + kvoff, 8192);
    const int R0 = 256 * (s & 1) + 64 * a + 4 * u, R1 = 128 * (s & 3) + 32 * a + 2 * u;
    const v4u xf = __builtin_amdgcn_raw_buffer_load_b128(rx, l * 16, 0, 0);
    const v2u x4 = __builtin_amdgcn_raw_buffer_load_b64(rx, R0 * 2, 0, 0);
    const unsigned x2 = __builtin_amdgcn_raw_buffer_load_b32(rx, R1 * 2, 0, 0);
    v4u wqr[4], w1r[2], kr[8], vr[8];
    const int qoff = (R0 * 4096 + 512 * g + 32 * (s >> 1) + 8 * cq) * 2;
#pragma unroll
    for (int i = 0; i < 4; ++i) wqr[i] = __builtin_amdgcn_raw_buffer_load_b128(rq, qoff, i * 8192, AUX_NT);
    const int off1 = (R1 * 1792 + 224 * g + 32 * (s >> 2) + 8 * cq) * 2;
#pragma unroll
    for (int i = 0; i < 2; ++i) w1r[i] = __builtin_amdgcn_raw_buffer_load_b128(r1, off1, i * 3584, AUX_NT);
#pragma unroll
    for (int i = 0; i < 8; ++i) kr[i] = __builtin_amdgcn_raw_buffer_load_b128(rk, c * 256 + h * 16 + (i & 3) * 64, (i >> 2) * 4096, AUX_NT);
#pragma unroll
    for (int j = 0; j < NV_EARLY; ++j) vr[j] = __builtin_amdgcn_raw_buffer_load_b128(rv, h * 1024 + c * 16 + (j & 3) * 256, (j >> 2) * 4096, AUX_NT);
    fence_sched();
    STAMP(1);
    float xv[8];
#pragma unroll
    for (int i = 0; i < 4; ++i) { xv[2 * i] = lo16(xf[i]); xv[2 * i + 1] = hi16(xf[i]); }
    float sm = ((xv[0] + xv[1]) + (xv[2] + xv[3])) + ((xv[4] + xv[5]) + (xv[6] + xv[7]));
    const float mean = wave_sum(sm) * (1.f / 512.f);
    float sq = 0.f;
#pragma unroll
    for (int i = 0; i < 8; ++i) { const float d = xv[i] - mean; sq = fmaf(d, d, sq); }
    const float var = wave_sum(sq) * (1.f / 512.f);
    const float mb = rbf(mean);
    const float rden = __builtin_amdgcn_rcpf(rbf(__builtin_amdgcn_sqrtf(rbf(rbf(var) + 1e-5f))));
    auto norm2 = [&](unsigned p) { const unsigned d = pk(lo16(p) - mb, hi16(p) - mb); return pk(lo16(d) * rden, hi16(d) * rden); };
    const unsigned xq0 = norm2(x4.x), xq1 = norm2(x4.y), xh = norm2(x2);
    {
      float acc[8];
#pragma unroll
      for (int t = 0; t < 4; ++t) {
        acc[2 * t] = dot2(perm_lo(wqr[2][t], wqr[3][t]), xq1, dot2(perm_lo(wqr[0][t], wqr[1][t]), xq0, 0.f));
        acc[2 * t + 1] = dot2(perm_hi(wqr[2][t], wqr[3][t]), xq1, dot2(perm_hi(wqr[0][t], wqr[1][t]), xq0, 0.f));
      }
      const f32x2 r = reduce8(acc);
      if ((l & 12) == 0) *(LAS f32x2*)(L + L_QP + a * 128 + (8 * cq + 2 * h) * 4) = r;
    }
    STAMP(2);
    barrier();
    {
      float acc[8];
#pragma unroll
      for (int t = 0; t < 4; ++t) {
        acc[2 * t] = dot2(perm_lo(w1r[0][t], w1r[1][t]), xh, 0.f);
        acc[2 * t + 1] = dot2(perm_hi(w1r[0][t], w1r[1][t]), xh, 0.f);
      }
      const f32x2 r = reduce8(acc);
      if ((l & 12) == 0) *(LAS f32x2*)(L + L_HP + a * 128 + (8 * cq + 2 * h) * 4) = r;
    }
    barrier();
    if (DELTA) __builtin_amdgcn_s_sleep(DELTA);
    STAMP(3);
    const v2u tg = *(const LAS v2u*)(L + L_TAG);
    const unsigned tag_lo = tg.x, tag_hi = tg.y;
    const int goff = WS_QG + (g * 1024 + 128 * a + 2 * l) * 8;
    v4u g0 = __builtin_amdgcn_raw_buffer_load_b128(rw, goff, 0, AUX_SC1);
    v4u g1 = __builtin_amdgcn_raw_buffer_load_b128(rw, goff, 4096, AUX_SC1);
#pragma unroll
    for (int j = NV_EARLY; j < 8; ++j) vr[j] = __builtin_amdgcn_raw_buffer_load_b128(rv, h * 1024 + c * 16 + (j & 3) * 256, (j >> 2) * 4096, AUX_NT);
    fence_sched();
    auto qbad = [&](v4u p, v4u q) {
      const unsigned hi = (p.y ^ tag_hi) | (p.w ^ tag_hi) | (q.y ^ tag_hi) | (q.w ^ tag_hi);
      const unsigned lo = ((p.x ^ tag_lo) | (p.z ^ tag_lo) | (q.x ^ tag_lo) | (q.z ^ tag_lo)) & 0xffu;
      return __builtin_amdgcn_ballot_w64((hi | lo) != 0u) != 0ull; };
    if (__builtin_expect(qbad(g0, g1), 0)) {
      STAMP(9);
      for (unsigned sp = 0; sp < SPIN_CAP; ++sp) {
        asm volatile("" ::: "memory");
        g0 = __builtin_amdgcn_raw_buffer_load_b128(rw, goff, 0, AUX_SC1);
        g1 = __builtin_amdgcn_raw_buffer_load_b128(rw, goff, 4096, AUX_SC1);
        if (!qbad(g0, g1)) break;
      }
    }
    *(LAS unsigned*)(L + L_QS + a * 272 + l * 4) = pk(asf(g0.x & 0xffffff00u) + asf(g1.x & 0xffffff00u), asf(g0.z & 0xffffff00u) + asf(g1.z & 0xffffff00u));
    STAMP(4);
    barrier();
    f32x4 sc[2] = {{0.f, 0.f, 0.f, 0.f}, {0.f, 0.f, 0.f, 0.f}};
#pragma unroll
    for (int t = 0; t < 4; ++t) {
      const v4u qf = *(const LAS v4u*)(L + L_QS + (c & 3) * 272 + t * 64 + h * 16);
      sc[0] = mfma32(kr[t], qf, sc[0]);
      sc[1] = mfma32(kr[4 + t], qf, sc[1]);
    }
    float sv[8];
#pragma unroll
    for (int i = 0; i < 4; ++i) {
      const unsigned r1_ = pk(sc[i >> 1][2 * (i & 1)], sc[i >> 1][2 * (i & 1) + 1]);
      const unsigned r2_ = pk(lo16(r1_) * 0.08838834764831845f, hi16(r1_) * 0.08838834764831845f);
      sv[2 * i] = lo16(r2_); sv[2 * i + 1] = hi16(r2_);
    }
    float m = fmaxf(fmaxf(fmaxf(sv[0], sv[1]), fmaxf(sv[2], sv[3])), fmaxf(fmaxf(sv[4], sv[5]), fmaxf(sv[6], sv[7])));
    m = sw32max(sw16max(m));
    constexpr float LOG2E = 1.4426950408889634f;
    float p[8], ls = 0.f;
#pragma unroll
    for (int i = 0; i < 8; ++i) { p[i] = __builtin_amdgcn_exp2f((sv[i] - m) * LOG2E); ls += p[i]; }
    ls = sw16add(ls, ls); ls = sw32add(ls, ls);
    if (l < 4) *(LAS f32x2*)(L + L_ST + l * 32 + a * 8) = f32x2{m * LOG2E, ls};
    const v4u pf = {pk(p[0], p[1]), pk(p[2], p[3]), pk(p[4], p[5]), pk(p[6], p[7])};
    STAMP(5);
    f32x4 oa[2] = {{0.f, 0.f, 0.f, 0.f}, {0.f, 0.f, 0.f, 0.f}};
#pragma unroll
    for (int ga = 0; ga < 4; ++ga) {
      const bool on = (c >> 2) == ga;
      const v4u pm = {on ? pf.x : 0u, on ? pf.y : 0u, on ? pf.z : 0u, on ? pf.w : 0u};
      const v4u v0 = {perm_lo(vr[0][ga], vr[1][ga]), perm_lo(vr[2][ga], vr[3][ga]), perm_lo(vr[4][ga], vr[5][ga]), perm_lo(vr[6][ga], vr[7][ga])};
      const v4u v1 = {perm_hi(vr[0][ga], vr[1][ga]), perm_hi(vr[2][ga], vr[3][ga]), perm_hi(vr[4][ga], vr[5][ga]), perm_hi(vr[6][ga], vr[7][ga])};
      oa[0] = mfma32(v0, pm, oa[0]);
      oa[1] = mfma32(v1, pm, oa[1]);
    }
    *(LAS f32x4*)(L + L_ML + a * 2048 + l * 16) = oa[0];
    *(LAS f32x4*)(L + L_ML + a * 2048 + 1024 + l * 16) = oa[1];
    STAMP(6);
    barrier();
    {
      const int qd = (l >> 3) & 3, kap = l & 7;
      const f32x4 s01 = *(const LAS f32x4*)(L + L_ST + a * 32), s23 = *(const LAS f32x4*)(L + L_ST + a * 32 + 16);
      const int src = L_ML + (kap & 1) * 1024 + (4 * (kap >> 1) + a + 16 * qd) * 16;
      const f32x4 o0 = *(const LAS f32x4*)(L + src), o1 = *(const LAS f32x4*)(L + src + 2048), o2 = *(const LAS f32x4*)(L + src + 4096), o3 = *(const LAS f32x4*)(L + src + 6144);
      const float M = fmaxf(fmaxf(s01.x, s01.z), fmaxf(s23.x, s23.z));
      float w0 = __builtin_amdgcn_exp2f(s01.x - M), w1_ = __builtin_amdgcn_exp2f(s01.z - M), w2_ = __builtin_amdgcn_exp2f(s23.x - M), w3 = __builtin_amdgcn_exp2f(s23.z - M);
      const float Ls = (w0 * s01.y + w1_ * s01.w) + (w2_ * s23.y + w3 * s23.w);
      const float inv = __builtin_amdgcn_rcpf(Ls);
      const unsigned lse = asu(M + __builtin_amdgcn_logf(Ls));
      w0 *= inv; w1_ *= inv; w2_ *= inv; w3 *= inv;
      const f32x4 o = (o0 * w0 + o1 * w1_) + (o2 * w2_ + o3 * w3);
      const v4u pv = {canon(__builtin_amdgcn_perm(asu(o.x), lse, 0x07060500u)), canon(__builtin_amdgcn_perm(asu(o.y), lse, 0x07060501u)),
                      canon(__builtin_amdgcn_perm(asu(o.z), lse, 0x07060502u)), canon(__builtin_amdgcn_perm(asu(o.w), lse, 0x07060503u))};
      if (l < 32) __builtin_amdgcn_raw_buffer_store_b128(pv, rw, WS_P2 + (((4 * g + a) * 4 + qd) * 32 + s) * 128 + kap * 16, 0, AUX_SC1);
    }
    STAMP(7);
  } else {
    const int rgl = s >> 1, ch = s & 1, rg = 16 * g + rgl;
    const v2u ep = __builtin_amdgcn_raw_buffer_load_b64(rw, 0, 0, 0);
    const v4u ones = {~0u, ~0u, ~0u, ~0u};
    __builtin_amdgcn_raw_buffer_store_b128(ones, rw, WS_P2 + ((16 * g + (l >> 3)) * 32 + s) * 128 + (l & 7) * 16, 0, AUX_SC1);
    __builtin_amdgcn_raw_buffer_store_b128(ones, rw, WS_P2 + ((16 * g + 8 + (l >> 3)) * 32 + s) * 128 + (l & 7) * 16, 0, AUX_SC1);
    __builtin_amdgcn_raw_buffer_store_b128(ones, rw, WS_P3 + (ch * 128 + rg) * 1024 + l * 16, 0, AUX_SC1);
    const int hpoff = WS_HP + (g * 4 + (s & 3)) * 1024 + (s >> 2) * 128;
    if (l < 8) __builtin_amdgcn_raw_buffer_store_b128(ones, rw, hpoff + l * 16, 0, AUX_SC1);
    asm volatile("s_waitcnt vmcnt(0)" ::: "memory");
    const u64 tag = (((u64)ep.y << 32) | ep.x) + 1;
    const unsigned tag_lo = (unsigned)tag & 0xffu, tag_hi = (unsigned)(tag >> 8);
    *(LAS v2u*)(L + L_TAG) = v2u{tag_lo, tag_hi};
    STAMP(1);
    barrier();
    {
      const LAS float* qp = (const LAS float*)(L + L_QP) + (l & 31);
      const float q = (qp[0] + qp[32]) + (qp[64] + qp[96]);
      const v2u gr = {(asu(q) & 0xffffff00u) | tag_lo, tag_hi};
      if (l < 32) __builtin_amdgcn_raw_buffer_store_b64(gr, rw, WS_QG + ((g * 2 + (s & 1)) * 512 + 32 * (s >> 1) + l) * 8, 0, AUX_SC1);
    }
    STAMP(2);
    barrier();
    {
      const LAS float* hp = (const LAS float*)(L + L_HP) + (l & 31);
      const float hv = (hp[0] + hp[32]) + (hp[64] + hp[96]);
      if (l < 32) __builtin_amdgcn_raw_buffer_store_b32(canon(asu(hv)), rw, hpoff + l * 4, 0, AUX_SC1);
    }
    if (CDELTA) __builtin_amdgcn_s_sleep(CDELTA);
    STAMP(3);
    const __amdgpu_buffer_rsrc_t ro = rsrc(wo, 4096 * 512 * 2), r2 = rsrc(w2, 1792 * 512 * 2), rx = rsrc(x, 1024);
    v4u wr[2][8], fr[2][4];
    const int ooff = ((32 * rg + 2 * h) * 512 + 256 * ch + 8 * c) * 2;
#pragma unroll
    for (int j = 0; j < 8; ++j)
#pragma unroll
      for (int cb = 0; cb < 2; ++cb) wr[cb][j] = __builtin_amdgcn_raw_buffer_load_b128(ro, ooff + cb * 256, (8 * (j & 3) + (j >> 2)) * 1024, AUX_NT);
#pragma unroll
    for (int j = 0; j < 4; ++j)
#pragma unroll
      for (int cb = 0; cb < 2; ++cb) fr[cb][j] = __builtin_amdgcn_raw_buffer_load_b128(r2, ((14 * rg + min(4 * h + j, 13)) * 512 + 256 * ch + 8 * c) * 2 + cb * 256, 0, AUX_NT);
    const int rpos = 4 * (b & 15) + (l & 3);
    const int rcol = 256 * ((b >> 4) & 1) + 128 * (rpos >> 5) + 8 * (rpos & 15) + 4 * ((rpos >> 4) & 1) + h;
    const unsigned xres = __builtin_amdgcn_raw_buffer_load_b16(rx, rcol * 2, 0, 0);
    fence_sched();
    STAMP(4);
    barrier();
    barrier();
    STAMP(5);
    f32x4 acc[4] = {{0.f, 0.f, 0.f, 0.f}, {0.f, 0.f, 0.f, 0.f}, {0.f, 0.f, 0.f, 0.f}, {0.f, 0.f, 0.f, 0.f}};
    {
      v4u hg[4];
      const int hoff = WS_HP + g * 4096 + (14 * rgl + 4 * h) * 4;
      for (unsigned sp = 0; sp < SPIN_CAP; ++sp) {
        asm volatile("" ::: "memory");
#pragma unroll
        for (int r = 0; r < 4; ++r) hg[r] = __builtin_amdgcn_raw_buffer_load_b128(rw, hoff, r * 1024, AUX_SC1);
        const unsigned m01 = max(max3u(hg[0].x, hg[1].x, hg[2].x), max3u(hg[3].x, hg[0].y, hg[1].y)), m01b = max3u(m01, hg[2].y, hg[3].y);
        const unsigned m23 = max(max3u(hg[0].z, hg[1].z, hg[2].z), max3u(hg[3].z, hg[0].w, hg[1].w)), m23b = max3u(m23, hg[2].w, hg[3].w);
        const bool bad = (m01b == ~0u) || (h < 3 && m23b == ~0u);
        if (__builtin_amdgcn_ballot_w64(bad) == 0ull) break;
      }
      float hv[4];
#pragma unroll
      for (int j = 0; j < 4; ++j) {
        const float t = rbf((asf(hg[0][j]) + asf(hg[1][j])) + (asf(hg[2][j]) + asf(hg[3][j])));
        hv[j] = t * __builtin_amdgcn_rcpf(1.f + __builtin_amdgcn_exp2f(-1.4426950408889634f * t));
      }
      const v2u hf = {pk(hv[0], hv[1]), h < 3 ? pk(hv[2], hv[3]) : 0u};
#pragma unroll
      for (int ga = 0; ga < 4; ++ga) {
        const bool on = (c >> 2) == ga;
        const v2u hm = {on ? hf.x : 0u, on ? hf.y : 0u};
        const int cb = ga >> 1;
#pragma unroll
        for (int e2 = 0; e2 < 2; ++e2) {
          const int t = 2 * (ga & 1) + e2;
          acc[2 * e2] = mfma16(hm, v2u{perm_lo(fr[cb][0][t], fr[cb][1][t]), perm_lo(fr[cb][2][t], fr[cb][3][t])}, acc[2 * e2]);
          acc[2 * e2 + 1] = mfma16(hm, v2u{perm_hi(fr[cb][0][t], fr[cb][1][t]), perm_hi(fr[cb][2][t], fr[cb][3][t])}, acc[2 * e2 + 1]);
        }
      }
    }
    v4u wt[2][8];
#pragma unroll
    for (int cb = 0; cb < 2; ++cb)
#pragma unroll
      for (int t = 0; t < 4; ++t) {
        wt[cb][2 * t] = v4u{perm_lo(wr[cb][0][t], wr[cb][1][t]), perm_lo(wr[cb][2][t], wr[cb][3][t]), perm_lo(wr[cb][4][t], wr[cb][5][t]), perm_lo(wr[cb][6][t], wr[cb][7][t])};
        wt[cb][2 * t + 1] = v4u{perm_hi(wr[cb][0][t], wr[cb][1][t]), perm_hi(wr[cb][2][t], wr[cb][3][t]), perm_hi(wr[cb][4][t], wr[cb][5][t]), perm_hi(wr[cb][6][t], wr[cb][7][t])};
      }
#pragma unroll
    for (int cb = 0; cb < 2; ++cb)
#pragma unroll
      for (int t = 0; t < 8; ++t) asm volatile("" : "+v"(wt[cb][t]));
    if (b < 32) {
      const int coff = WS_QG + (((l >> 4) * 2 + (b >> 4)) * 512 + 32 * (l & 15)) * 8;
      for (unsigned sp = 0; sp < SPIN_CAP; ++sp) {
        asm volatile("" ::: "memory");
        const v2u t0 = __builtin_amdgcn_raw_buffer_load_b64(rw, coff, 0, AUX_SC1), t1 = __builtin_amdgcn_raw_buffer_load_b64(rw, coff, 4 * 8192, AUX_SC1);
        const unsigned bad = (t0.y ^ tag_hi) | (t1.y ^ tag_hi) | (((t0.x ^ tag_lo) | (t1.x ^ tag_lo)) & 0xffu);
        if (__builtin_amdgcn_ballot_w64(bad != 0u) == 0ull) break;
      }
    }
    STAMP(6);
    v4u pg[4];
    const int poff = WS_P2 + (rg * 32 + ((l >> 1) & 7)) * 128 + (2 * h + (l & 1)) * 16;
    for (unsigned sp = 0; sp < SPIN_CAP; ++sp) {
        asm volatile("" ::: "memory");
#pragma unroll
      for (int i = 0; i < 4; ++i) pg[i] = __builtin_amdgcn_raw_buffer_load_b128(rw, poff, i * 1024, AUX_SC1);
      const unsigned mx = max(max(max4(pg[0]), max4(pg[1])), max(max4(pg[2]), max4(pg[3])));
      if (__builtin_amdgcn_ballot_w64(mx == ~0u) == 0ull) break;
    }
    STAMP(7);
    float lse[4];
#pragma unroll
    for (int i = 0; i < 4; ++i)
      lse[i] = asf(__builtin_amdgcn_perm(__builtin_amdgcn_perm(pg[i].w, pg[i].z, 0x0c0c0400u), __builtin_amdgcn_perm(pg[i].y, pg[i].x, 0x0c0c0400u), 0x05040100u));
    float M = fmaxf(fmaxf(lse[0], lse[1]), fmaxf(lse[2], lse[3]));
    M = fmaxf(M, dpp<ROR8>(M)); M = fmaxf(M, dpp<ROR4>(M)); M = fmaxf(M, dpp<ROR2>(M));
    float ov[5] = {0.f, 0.f, 0.f, 0.f, 0.f};
#pragma unroll
    for (int i = 0; i < 4; ++i) {
      const float w = __builtin_amdgcn_exp2f(lse[i] - M);
      ov[4] += w;
#pragma unroll
      for (int e = 0; e < 4; ++e) ov[e] = fmaf(w, asf(pg[i][e]), ov[e]);
    }
#pragma unroll
    for (int e = 0; e < 5; ++e) { ov[e] += dpp<ROR8>(ov[e]); ov[e] += dpp<ROR4>(ov[e]); ov[e] += dpp<ROR2>(ov[e]); }
    const float inv = __builtin_amdgcn_rcpf(ov[4]);
    const unsigned oa = pk(ov[0] * inv, ov[1] * inv), ob = pk(ov[2] * inv, ov[3] * inv);
    const v4u of = {oa, ob, dppu<XOR1>(oa), dppu<XOR1>(ob)};
#pragma unroll
    for (int ga = 0; ga < 4; ++ga) {
      const bool on = (c >> 2) == ga;
      const v4u om = {on ? of.x : 0u, on ? of.y : 0u, on ? of.z : 0u, on ? of.w : 0u};
#pragma unroll
      for (int e = 0; e < 4; ++e) acc[e] = mfma32(om, wt[ga >> 1][4 * (ga & 1) + e], acc[e]);
    }
    const v4u p3 = {canon(asu(acc[0].x)), canon(asu(acc[1].x)), canon(asu(acc[2].x)), canon(asu(acc[3].x))};
    __builtin_amdgcn_raw_buffer_store_b128(p3, rw, WS_P3 + (ch * 128 + rg) * 1024 + l * 16, 0, AUX_SC1);
    STAMP(8);
    if (b < 32) {
      v4u fg[8];
      const int foff = WS_P3 + ((b >> 4) * 128 + (l >> 2)) * 1024 + rpos * 16;
      for (unsigned sp = 0; sp < SPIN_CAP; ++sp) {
        asm volatile("" ::: "memory");
#pragma unroll
        for (int i = 0; i < 8; ++i) fg[i] = __builtin_amdgcn_raw_buffer_load_b128(rw, foff, i * 16384, AUX_SC1);
        unsigned mx = 0;
#pragma unroll
        for (int i = 0; i < 8; ++i) mx = max(mx, max4(fg[i]));
        if (__builtin_amdgcn_ballot_w64(mx == ~0u) == 0ull) break;
      }
      STAMP(9);
      float t[4];
#pragma unroll
      for (int e = 0; e < 4; ++e)
        t[e] = ((asf(fg[0][e]) + asf(fg[1][e])) + (asf(fg[2][e]) + asf(fg[3][e]))) + ((asf(fg[4][e]) + asf(fg[5][e])) + (asf(fg[6][e]) + asf(fg[7][e])));
      float r = sw16add(sw32add(t[0], t[2]), sw32add(t[1], t[3]));
      r += dpp<ROR8>(r); r += dpp<ROR4>(r);
      const unsigned res = pk(lo16(xres) + r, 0.f);
      if ((l & 12) == 0) ((GAS u16*)out)[rcol] = (u16)res;
      if (b == 0 && l == 0) __hip_atomic_store((GAS u64*)ws, tag, __ATOMIC_RELAXED, __HIP_MEMORY_SCOPE_AGENT);
      STAMP(10);
    }
  }
  if (b == 0 && wave == 0) { u64 t = __hip_atomic_load((GAS u64*)(ws + 1024), __ATOMIC_RELAXED, __HIP_MEMORY_SCOPE_AGENT); t = __builtin_amdgcn_readfirstlane((unsigned)t) | ((u64)__builtin_amdgcn_readfirstlane((unsigned)(t >> 32)) << 32); unsigned n = 0; while (rt() < t + 3000 && ++n < 100000) __builtin_amdgcn_s_sleep(1); }
}

__global__ void k_pre(unsigned char* ws) { u64 t = rt(); if (blockIdx.x == 0 && threadIdx.x == 0) __hip_atomic_store((GAS u64*)(ws + 1024), t, __ATOMIC_RELAXED, __HIP_MEMORY_SCOPE_AGENT); }
__global__ void k_post(unsigned char* ws) { u64 t = __hip_atomic_load((GAS u64*)(ws + 1024), __ATOMIC_RELAXED, __HIP_MEMORY_SCOPE_AGENT); t = __builtin_amdgcn_readfirstlane((unsigned)t) | ((u64)__builtin_amdgcn_readfirstlane((unsigned)(t >> 32)) << 32); unsigned n = 0; while (rt() < t + 3000 && ++n < 100000) __builtin_amdgcn_s_sleep(1); }
extern "C" void kernel_launch(void* const* d_in, const int* in_sizes, int n_in, void* d_out, int out_size, void* d_ws, size_t ws_size, hipStream_t stream) {
  k_pre<<<256, 64, 0, stream>>>((unsigned char*)d_ws);
  fused_decode<<<256, 320, 0, stream>>>((const u16*)d_in[0], (const u16*)d_in[1], (const u16*)d_in[2], (const u16*)d_in[3], (const u16*)d_in[4],
                                        (const u16*)d_in[6], (const u16*)d_in[7], (u16*)d_out, (unsigned char*)d_ws);
}
```

```cpp
#include <hip/hip_runtime.h>

typedef unsigned short u16;

__global__ void __launch_bounds__(448, 2)
fused_decode(const u16* x, const u16* Kc, const u16* Vc, const u16* wq, unsigned char* ws, const u16* wo, const u16* w1, const u16* w2, u16* out) {
  __shared__ __attribute__((aligned(16))) unsigned char lds_[44416];
  asm volatile(
R"ASM(
.Lk_start:
s_load_dwordx16 s[64:79], s[0:1], 0x0
s_mov_b32 s16, s2
v_readfirstlane_b32 s17, v0
s_nop 0
s_cmp_lt_u32 s17, 64
s_cbranch_scc0 .Lk_notq
s_ashr_i32 s2, s16, 3
s_and_b32 s18, s2, 3
s_lshl_b32 s2, s2, 6
s_and_b32 s17, s16, 7
s_and_b32 s2, s2, 0x1c0
s_lshl_b32 s3, s16, 1
v_and_or_b32 v4, v0, 56, s2
s_lshl_b32 s2, s17, 11
s_andn2_b32 s3, s3, 63
s_add_i32 s19, s3, s2
s_mul_i32 s2, s17, 0xfffffa00
v_lshlrev_b32_e32 v82, 3, v0
v_lshlrev_b32_e32 v2, 1, v0
s_add_i32 s2, s19, s2
v_and_b32_e32 v80, 56, v82
v_and_b32_e32 v1, 63, v0
v_and_b32_e32 v2, 0x70, v2
v_or_b32_e32 v6, s2, v80
v_lshl_or_b32 v3, s18, 7, v2
v_lshlrev_b32_e32 v2, 4, v1
v_lshlrev_b32_e32 v6, 1, v6
v_lshlrev_b32_e32 v5, 1, v3
v_lshlrev_b32_e32 v4, 1, v4
v_lshl_add_u32 v3, v3, 13, v6
v_mov_b32_e32 v6, v2
s_mov_b32 s2, 0
s_mov_b32 s3, 0x20000
s_movk_i32 s22, 0x400
s_mov_b32 s23, s3
s_mov_b32 s2, 0xe1000
s_waitcnt lgkmcnt(0)
s_mov_b64 s[20:21], s[64:65]
s_mov_b64 s[0:1], s[72:73]
s_and_b32 s21, s21, 0xffff
s_and_b32 s1, s1, 0xffff
s_memrealtime s[4:5]
buffer_load_dwordx4 v[84:87], v6, s[20:23], 0 offen
buffer_load_dwordx4 v[76:79], v5, s[20:23], 0 offen
buffer_load_dwordx4 v[72:75], v5, s[20:23], 0 offen offset:16
buffer_load_dword v83, off, s[0:3], 0
s_mov_b32 s9, s71
s_and_b32 s9, s9, 0xffff
s_mov_b32 s10, 0x400000
s_mov_b32 s11, s3
s_movk_i32 s6, 0x2000
s_mov_b32 s8, s70
buffer_load_dwordx4 v[64:67], v3, s[8:11], 0 offen nt
buffer_load_dwordx4 v[68:71], v3, s[8:11], s6 offen nt
s_movk_i32 s6, 0x4000
s_movk_i32 s7, 0x6000
buffer_load_dwordx4 v[56:59], v3, s[8:11], s6 offen nt
buffer_load_dwordx4 v[60:63], v3, s[8:11], s7 offen nt
s_mov_b32 s6, 0x8000
s_mov_b32 s7, 0xa000
buffer_load_dwordx4 v[48:51], v3, s[8:11], s6 offen nt
buffer_load_dwordx4 v[52:55], v3, s[8:11], s7 offen nt
s_mov_b32 s6, 0xc000
s_mov_b32 s7, 0xe000
buffer_load_dwordx4 v[40:43], v3, s[8:11], s6 offen nt
buffer_load_dwordx4 v[44:47], v3, s[8:11], s7 offen nt
s_mov_b32 s6, 0x10000
s_mov_b32 s7, 0x12000
buffer_load_dwordx4 v[32:35], v3, s[8:11], s6 offen nt
buffer_load_dwordx4 v[36:39], v3, s[8:11], s7 offen nt
s_mov_b32 s6, 0x14000
s_mov_b32 s7, 0x16000
buffer_load_dwordx4 v[24:27], v3, s[8:11], s6 offen nt
buffer_load_dwordx4 v[28:31], v3, s[8:11], s7 offen nt
s_mov_b32 s6, 0x18000
s_mov_b32 s7, 0x1a000
buffer_load_dwordx4 v[16:19], v3, s[8:11], s6 offen nt
buffer_load_dwordx4 v[20:23], v3, s[8:11], s7 offen nt
s_mov_b32 s6, 0x1c000
s_mov_b32 s7, 0x1e000
buffer_load_dwordx4 v[8:11], v3, s[8:11], s6 offen nt
buffer_load_dwordx4 v[12:15], v3, s[8:11], s7 offen nt
s_lshl_b32 s6, s18, 9
s_add_i32 s19, s19, s6
buffer_load_dwordx4 v[4:7], v4, s[20:23], 0 offen
s_waitcnt lgkmcnt(0)
v_mov_b32_e32 v3, 0
v_mov_b32_e32 v81, s4
ds_write_b32 v3, v81 offset:1392
s_waitcnt lgkmcnt(0)
s_mov_b32 s12, s74
s_mov_b32 s13, s75
s_mov_b32 s14, s76
s_mov_b32 s15, s77
s_barrier
s_branch .Lk_q2
.Lk_notq:
s_getpc_b64 s[18:19]
.Lk_pc:
s_sub_u32 s18, s18, .Lk_pc-.Lk_start
s_subb_u32 s19, s19, 0
s_lshr_b32 s20, s16, 3
s_mulk_i32 s20, 0x300
s_lshl_b32 s21, s17, 1
s_add_i32 s20, s20, s21
s_addk_i32 s20, 0xff80
s_min_u32 s20, s20, .Lk_end-.Lk_start-4
v_mov_b32_e32 v1, s20
global_load_dword v160, v1, s[18:19]
s_cmpk_lt_u32 s17, 0x140
s_cbranch_scc1 .Lk_a
v_and_b32_e32 v1, 63, v0
v_lshlrev_b32_e32 v1, 2, v1
v_mov_b32_e32 v2, 0
ds_write_b32 v1, v2 offset:1088
s_waitcnt lgkmcnt(0)
s_barrier
s_cmpk_lt_u32 s17, 0x180
s_cbranch_scc1 .Lk_c
s_branch .Lk_f
.Lk_a:
s_ashr_i32 s17, s16, 3
v_readfirstlane_b32 s6, v0
s_lshl_b32 s19, s17, 12
s_lshr_b32 s18, s6, 6
s_add_i32 s6, s19, 0x1000
s_and_b32 s6, s6, 0x1f000
s_or_b32 s26, s6, 0x20000
s_add_i32 s6, s19, 0x2000
s_and_b32 s6, s6, 0x1f000
s_or_b32 s25, s6, 0x40000
s_add_i32 s6, s19, 0x3000
s_and_b32 s6, s6, 0x1f000
s_and_b32 s28, s16, 7
s_or_b32 s24, s6, 0x60000
s_lshl_b32 s6, s28, 24
v_lshl_or_b32 v2, v0, 4, s6
s_and_b32 s27, s19, 0x1f000
v_add_u32_e32 v52, 0xeffc00, v2
v_and_b32_e32 v1, 63, v0
v_bfe_u32 v50, v0, 4, 2
s_mov_b32 s7, 0x20000
v_and_b32_e32 v51, 15, v0
s_mov_b32 s16, 0
s_brev_b32 s6, 16
s_mov_b64 s[10:11], s[6:7]
s_waitcnt lgkmcnt(0)
s_mov_b64 s[0:1], s[68:69]
s_mov_b64 s[20:21], s[72:73]
s_and_b32 s5, s67, 0xffff
s_mov_b32 s4, s66
s_mov_b64 s[8:9], s[4:5]
s_mov_b32 s2, s64
s_mov_b32 s3, s65
s_mov_b32 s12, s74
s_mov_b32 s13, s75
s_mov_b32 s14, s76
s_mov_b32 s15, s77
s_barrier
buffer_load_dwordx4 v[18:21], v52, s[8:11], s27 offen nt
buffer_load_dwordx4 v[22:25], v52, s[8:11], s26 offen nt
buffer_load_dwordx4 v[26:29], v52, s[8:11], s25 offen nt
buffer_load_dwordx4 v[30:33], v52, s[8:11], s24 offen nt
s_and_b32 s21, s21, 0xffff
s_mov_b32 s22, 0xe1000
s_mov_b32 s23, s7
s_add_i32 s2, s19, 0x4000
s_and_b32 s2, s2, 0x1f000
s_or_b32 s13, s2, 0x80000
s_add_i32 s2, s19, 0x5000
s_and_b32 s2, s2, 0x1f000
s_or_b32 s14, s2, 0xa0000
s_add_i32 s2, s19, 0x6000
s_and_b32 s2, s2, 0x1f000
s_addk_i32 s19, 0x7000
s_or_b32 s5, s2, 0xc0000
s_and_b32 s2, s19, 0x1f000
s_or_b32 s12, s2, 0xe0000
s_add_i32 s4, s18, -1
s_and_b32 s1, s1, 0xffff
s_mov_b32 s2, s6
s_mov_b32 s3, s7
v_and_b32_e32 v53, 3, v0
v_and_b32_e32 v2, 48, v0
s_movk_i32 s6, 0x110
v_mad_u32_u24 v139, v53, s6, v2
s_lshl_b32 s6, s4, 11
v_lshl_or_b32 v1, v1, 4, s6
v_add_u32_e32 v133, 0xd80, v1
v_lshlrev_b32_e32 v1, 5, v0
v_and_b32_e32 v1, 0x60, v1
v_add_u32_e32 v1, 0x440, v1
v_lshlrev_b32_e32 v2, 1, v0
v_and_or_b32 v137, v2, 24, v1
v_and_b32_e32 v2, 1, v0
v_lshl_add_u32 v138, s4, 3, v1
v_lshlrev_b32_e32 v1, 9, v0
v_lshlrev_b32_e32 v3, 3, v2
s_movk_i32 s7, 0x400
)ASM"
R"ASM(
v_and_or_b32 v1, v1, s7, v3
v_and_b32_e32 v3, 60, v0
s_movk_i32 s6, 0xd80
v_add_lshl_u32 v3, v3, s4, 4
v_add3_u32 v131, v1, v3, s6
s_lshl_b32 s6, s28, 4
s_lshl_b32 s7, s4, 2
s_add_i32 s6, s7, s6
v_or_b32_e32 v1, s6, v50
s_lshl_b32 s6, s17, 7
s_add_i32 s6, s6, 0x21000
v_lshl_or_b32 v3, v51, 3, s6
v_lshl_add_u32 v130, v1, 12, v3
v_mov_b32_e32 v1, 0x7060100
v_mov_b32_e32 v3, 0x5040100
v_cmp_eq_u32_e32 vcc, 0, v2
s_mov_b32 s17, s16
s_mov_b32 s18, s16
v_cndmask_b32_e32 v136, v1, v3, vcc
s_mov_b32 s19, s16
v_mov_b64_e32 v[2:3], s[16:17]
v_mov_b64_e32 v[14:15], s[16:17]
v_mov_b64_e32 v[10:11], s[16:17]
v_mov_b64_e32 v[6:7], s[16:17]
v_mov_b64_e32 v[4:5], s[18:19]
v_mov_b32_e32 v34, 0
v_mov_b32_e32 v134, s7
v_mov_b32_e32 v132, 0
v_mov_b32_e32 v135, 1
v_mov_b64_e32 v[16:17], s[18:19]
v_mov_b64_e32 v[12:13], s[18:19]
v_mov_b64_e32 v[8:9], s[18:19]
ds_read_b32 v34, v34 offset:1392
s_mul_i32 s16, s4, 0x1ffc
v_mov_b32_e32 v1, s7
s_add_i32 s7, s7, s16
v_lshlrev_b32_e32 v56, 2, v50
s_waitcnt lgkmcnt(0)
v_readfirstlane_b32 s6, v34
v_lshlrev_b32_e32 v34, 6, v0
v_and_b32_e32 v57, 0x300, v34
v_mov_b32_e32 v54, 1
s_add_i32 s15, s6, 0x6c
v_lshl_add_u32 v55, v50, 8, s7
s_branch 2f
1:
s_sleep 1
2:
s_memrealtime vcc
s_waitcnt lgkmcnt(0)
s_sub_i32 vcc_lo, vcc_lo, s15
s_cmp_lt_i32 vcc_lo, -8
s_cbranch_scc1 1b
s_cmp_lt_i32 vcc_lo, 0
s_cbranch_scc1 2b
buffer_load_dwordx4 v[34:37], v52, s[8:11], s13 offen nt
s_add_i32 s15, s6, 0x7c
s_branch 2f
1:
s_sleep 1
2:
s_memrealtime vcc
s_waitcnt lgkmcnt(0)
s_sub_i32 vcc_lo, vcc_lo, s15
s_cmp_lt_i32 vcc_lo, -8
s_cbranch_scc1 1b
s_cmp_lt_i32 vcc_lo, 0
s_cbranch_scc1 2b
buffer_load_dwordx4 v[38:41], v52, s[8:11], s14 offen nt
s_add_i32 s15, s6, 0xa4
s_branch 2f
1:
s_sleep 1
2:
s_memrealtime vcc
s_waitcnt lgkmcnt(0)
s_sub_i32 vcc_lo, vcc_lo, s15
s_cmp_lt_i32 vcc_lo, -8
s_cbranch_scc1 1b
s_cmp_lt_i32 vcc_lo, 0
s_cbranch_scc1 2b
buffer_load_dwordx4 v[42:45], v52, s[8:11], s5 offen nt
v_bitop3_b32 v46, v56, v0, 15 bitop3:0x78
v_lshl_add_u32 v58, v46, 4, v55
s_waitcnt vmcnt(6)
ds_write_b128 v58, v[18:21] offset:11648
s_waitcnt vmcnt(6)
s_add_i32 s15, s6, 0xb4
s_branch 2f
1:
s_sleep 1
2:
s_memrealtime vcc
s_waitcnt lgkmcnt(0)
s_sub_i32 vcc_lo, vcc_lo, s15
s_cmp_lt_i32 vcc_lo, -8
s_cbranch_scc1 1b
s_cmp_lt_i32 vcc_lo, 0
s_cbranch_scc1 2b
buffer_load_dwordx4 v[46:49], v52, s[8:11], s12 offen nt
v_bitop3_b32 v18, v56, v51, 1 bitop3:0x36
v_lshl_add_u32 v18, v18, 4, v55
s_waitcnt vmcnt(6)
ds_write_b128 v18, v[22:25] offset:12672
s_waitcnt vmcnt(6)
s_add_i32 s8, s6, 0xc4
s_branch 2f
1:
s_sleep 1
2:
s_memrealtime vcc
s_waitcnt lgkmcnt(0)
s_sub_i32 vcc_lo, vcc_lo, s8
s_cmp_lt_i32 vcc_lo, -8
s_cbranch_scc1 1b
s_cmp_lt_i32 vcc_lo, 0
s_cbranch_scc1 2b
buffer_load_dwordx4 v[74:77], v52, s[0:3], s27 offen nt
v_bitop3_b32 v19, v56, v51, 2 bitop3:0x36
v_lshl_add_u32 v22, v19, 4, v55
s_waitcnt vmcnt(6)
ds_write_b128 v22, v[26:29] offset:13696
s_waitcnt vmcnt(6)
s_add_i32 s8, s6, 0xd4
s_branch 2f
1:
s_sleep 1
2:
s_memrealtime vcc
s_waitcnt lgkmcnt(0)
s_sub_i32 vcc_lo, vcc_lo, s8
s_cmp_lt_i32 vcc_lo, -8
s_cbranch_scc1 1b
s_cmp_lt_i32 vcc_lo, 0
s_cbranch_scc1 2b
buffer_load_dwordx4 v[78:81], v52, s[0:3], s26 offen nt
v_bitop3_b32 v19, v56, v51, 3 bitop3:0x36
v_lshl_add_u32 v26, v19, 4, v55
s_waitcnt vmcnt(6)
ds_write_b128 v26, v[30:33] offset:14720
s_waitcnt vmcnt(6)
s_add_i32 s8, s6, 0xe4
s_branch 2f
1:
s_sleep 1
2:
s_memrealtime vcc
s_waitcnt lgkmcnt(0)
s_sub_i32 vcc_lo, vcc_lo, s8
s_cmp_lt_i32 vcc_lo, -8
s_cbranch_scc1 1b
s_cmp_lt_i32 vcc_lo, 0
s_cbranch_scc1 2b
buffer_load_dwordx4 v[66:69], v52, s[0:3], s25 offen nt
s_waitcnt vmcnt(6)
ds_write_b128 v58, v[34:37] offset:15744
s_waitcnt vmcnt(6)
s_add_i32 s8, s6, 0xf4
s_branch 2f
1:
s_sleep 1
2:
s_memrealtime vcc
s_waitcnt lgkmcnt(0)
s_sub_i32 vcc_lo, vcc_lo, s8
s_cmp_lt_i32 vcc_lo, -8
s_cbranch_scc1 1b
s_cmp_lt_i32 vcc_lo, 0
s_cbranch_scc1 2b
buffer_load_dwordx4 v[70:73], v52, s[0:3], s24 offen nt
s_waitcnt vmcnt(6)
ds_write_b128 v18, v[38:41] offset:16768
s_waitcnt vmcnt(6)
s_add_i32 s8, s6, 0x104
s_branch 2f
1:
s_sleep 1
2:
s_memrealtime vcc
s_waitcnt lgkmcnt(0)
s_sub_i32 vcc_lo, vcc_lo, s8
s_cmp_lt_i32 vcc_lo, -8
s_cbranch_scc1 1b
s_cmp_lt_i32 vcc_lo, 0
s_cbranch_scc1 2b
buffer_load_dwordx4 v[18:21], v52, s[0:3], s13 offen nt
s_waitcnt vmcnt(6)
ds_write_b128 v22, v[42:45] offset:17792
s_waitcnt vmcnt(6)
s_add_i32 s8, s6, 0x114
s_branch 2f
1:
s_sleep 1
2:
s_memrealtime vcc
s_waitcnt lgkmcnt(0)
s_sub_i32 vcc_lo, vcc_lo, s8
s_cmp_lt_i32 vcc_lo, -8
s_cbranch_scc1 1b
s_cmp_lt_i32 vcc_lo, 0
)ASM"
R"ASM(
s_cbranch_scc1 2b
buffer_load_dwordx4 v[22:25], v52, s[0:3], s14 offen nt
s_waitcnt vmcnt(6)
ds_write_b128 v26, v[46:49] offset:18816
v_lshlrev_b32_e32 v26, 10, v53
v_add3_u32 v26, s7, v57, v26
v_bitop3_b32 v0, v50, v0, 15 bitop3:0x78
v_lshl_add_u32 v0, v0, 4, v26
v_bitop3_b32 v27, v50, v51, 4 bitop3:0x36
v_bitop3_b32 v28, v50, v51, 8 bitop3:0x36
v_bitop3_b32 v29, v50, v51, 12 bitop3:0x36
v_lshl_add_u32 v27, v27, 4, v26
v_lshl_add_u32 v28, v28, 4, v26
v_lshl_add_u32 v26, v29, 4, v26
ds_read_b128 v[82:85], v0 offset:11648
ds_read_b128 v[98:101], v0 offset:15744
ds_read_b128 v[86:89], v27 offset:11648
ds_read_b128 v[102:105], v27 offset:15744
ds_read_b128 v[90:93], v28 offset:11648
ds_read_b128 v[106:109], v28 offset:15744
ds_read_b128 v[94:97], v26 offset:11648
ds_read_b128 v[110:113], v26 offset:15744
s_waitcnt lgkmcnt(7)
s_waitcnt lgkmcnt(5)
s_waitcnt lgkmcnt(3)
s_waitcnt lgkmcnt(1)
s_waitcnt lgkmcnt(0)
s_waitcnt vmcnt(6)
s_addk_i32 s6, 0x124
s_branch 2f
1:
s_sleep 1
2:
s_memrealtime vcc
s_waitcnt lgkmcnt(0)
s_sub_i32 vcc_lo, vcc_lo, s6
s_cmp_lt_i32 vcc_lo, -8
s_cbranch_scc1 1b
s_cmp_lt_i32 vcc_lo, 0
s_cbranch_scc1 2b
buffer_load_dwordx4 v[26:29], v52, s[0:3], s5 offen nt
buffer_load_dwordx4 v[30:33], v52, s[0:3], s12 offen nt
ds_write_b32 v1, v54 offset:1264
s_nop 0
v_mov_b32_e32 v34, 0xffffff
.La_1:
ds_read_b64 v[0:1], v132 offset:1216
ds_read_b128 v[126:129], v139
ds_read_b128 v[122:125], v139 offset:64
ds_read_b128 v[118:121], v139 offset:128
ds_read_b128 v[114:117], v139 offset:192
v_subrev_co_u32_e32 v34, vcc, 1, v34
s_waitcnt lgkmcnt(4)
v_readfirstlane_b32 s0, v0
s_cmp_lg_u32 s0, 0
s_cselect_b64 s[0:1], -1, 0
s_or_b64 s[0:1], s[0:1], vcc
s_andn2_b64 vcc, exec, s[0:1]
s_waitcnt lgkmcnt(0)
s_cbranch_vccnz .La_1
s_setprio 2
v_mfma_f32_16x16x32_bf16 v[82:85], v[82:85], v[126:129], 0
v_mfma_f32_16x16x32_bf16 v[98:101], v[98:101], v[126:129], 0
v_mfma_f32_16x16x32_bf16 v[82:85], v[86:89], v[122:125], v[82:85]
v_mfma_f32_16x16x32_bf16 v[86:89], v[102:105], v[122:125], v[98:101]
v_mfma_f32_16x16x32_bf16 v[82:85], v[90:93], v[118:121], v[82:85]
v_mfma_f32_16x16x32_bf16 v[86:89], v[106:109], v[118:121], v[86:89]
v_mfma_f32_16x16x32_bf16 v[82:85], v[94:97], v[114:117], v[82:85]
v_mfma_f32_16x16x32_bf16 v[86:89], v[110:113], v[114:117], v[86:89]
s_mov_b32 s0, 0x5040100
s_mov_b32 s1, 0x7060302
s_waitcnt vmcnt(6)
v_perm_b32 v62, v78, v74, s0
v_perm_b32 v34, v78, v74, s1
v_perm_b32 v46, v79, v75, s0
v_perm_b32 v38, v79, v75, s1
v_perm_b32 v50, v80, v76, s0
v_perm_b32 v42, v80, v76, s1
v_perm_b32 v58, v81, v77, s0
v_perm_b32 v54, v81, v77, s1
v_cvt_pk_bf16_f32 v0, v82, v83
v_cvt_pk_bf16_f32 v35, v84, v85
s_mov_b32 s2, 0x3fb8aa3b
s_nop 0
v_mov_b32_dpp v0, v35 quad_perm:[0,1,2,3] row_mask:0xf bank_mask:0x2
v_cvt_pk_bf16_f32 v35, v86, v87
s_nop 1
v_mov_b32_dpp v0, v35 quad_perm:[0,1,2,3] row_mask:0xf bank_mask:0x4
v_cvt_pk_bf16_f32 v35, v88, v89
s_nop 1
v_mov_b32_dpp v0, v35 quad_perm:[0,1,2,3] row_mask:0xf bank_mask:0x8
v_lshlrev_b32_e32 v35, 16, v0
v_and_b32_e32 v0, 0xffff0000, v0
v_mul_f32_e32 v35, 0x3db504f3, v35
v_mul_f32_e32 v0, 0x3db504f3, v0
v_cvt_pk_bf16_f32 v0, v35, v0
v_lshlrev_b32_e32 v35, 16, v0
v_and_b32_e32 v39, 0xffff0000, v0
v_max_f32_e32 v0, v35, v39
s_nop 1
v_max_f32_dpp v0, v0, v0 row_ror:4 row_mask:0xf bank_mask:0xf bound_ctrl:1
s_nop 1
v_max_f32_dpp v0, v0, v0 row_ror:8 row_mask:0xf bank_mask:0xf bound_ctrl:1
v_mov_b32_e32 v43, v0
s_nop 1
v_permlane16_swap_b32_e32 v0, v43
v_max_f32_e32 v0, v0, v43
v_mov_b32_e32 v43, v0
s_nop 1
v_permlane32_swap_b32_e32 v0, v43
v_max_f32_e32 v0, v0, v43
v_mul_f32_e32 v74, 0x3fb8aa3b, v0
v_fma_f32 v0, v35, s2, -v74
v_fma_f32 v35, v39, s2, -v74
v_exp_f32_e32 v0, v0
v_exp_f32_e32 v78, v35
s_nop 0
v_add_f32_e32 v35, v0, v78
s_nop 1
v_add_f32_dpp v35, v35, v35 row_ror:8 row_mask:0xf bank_mask:0xf bound_ctrl:1
s_nop 1
v_add_f32_dpp v35, v35, v35 row_ror:4 row_mask:0xf bank_mask:0xf bound_ctrl:1
v_mov_b32_e32 v39, v35
s_nop 1
v_permlane16_swap_b32_e32 v35, v39
v_add_f32_e32 v35, v35, v39
v_mov_b32_e32 v39, v35
s_nop 1
v_permlane32_swap_b32_e32 v35, v39
v_add_f32_e32 v35, v35, v39
v_or_b32_e32 v75, 1, v35
ds_write_b64 v138, v[74:75]
ds_read_b64 v[76:77], v137
s_waitcnt vmcnt(4)
v_perm_b32 v63, v70, v66, s0
v_perm_b32 v35, v70, v66, s1
v_perm_b32 v47, v71, v67, s0
v_perm_b32 v39, v71, v67, s1
v_perm_b32 v51, v72, v68, s0
v_perm_b32 v43, v72, v68, s1
v_perm_b32 v59, v73, v69, s0
v_perm_b32 v55, v73, v69, s1
s_waitcnt lgkmcnt(0)
v_cmp_eq_u32_e32 vcc, 0, v77
s_cbranch_vccz .La_6
s_setprio 0
v_mov_b32_e32 v36, 0xffffff
.La_4:
ds_read_b64 v[76:77], v137
v_subrev_co_u32_e32 v36, vcc, 1, v36
s_waitcnt lgkmcnt(0)
v_cmp_eq_u32_e64 s[0:1], 0, v77
s_cmp_eq_u64 s[0:1], 0
s_cselect_b64 s[0:1], -1, 0
s_or_b64 s[0:1], s[0:1], vcc
s_andn2_b64 vcc, exec, s[0:1]
s_cbranch_vccnz .La_4
s_setprio 2
.La_6:
v_max_f32_dpp v36, v76, v76 row_ror:4 row_mask:0xf bank_mask:0xf bound_ctrl:1
s_nop 1
v_max_f32_dpp v36, v36, v36 row_ror:8 row_mask:0xf bank_mask:0xf bound_ctrl:1
v_sub_f32_e32 v37, v76, v36
v_exp_f32_e32 v37, v37
v_sub_f32_e32 v40, v74, v36
v_exp_f32_e32 v40, v40
v_mul_f32_e32 v37, v77, v37
s_nop 1
v_add_f32_dpp v37, v37, v37 row_ror:8 row_mask:0xf bank_mask:0xf bound_ctrl:1
s_nop 1
v_add_f32_dpp v37, v37, v37 row_ror:4 row_mask:0xf bank_mask:0xf bound_ctrl:1
v_rcp_f32_e32 v41, v37
v_log_f32_e32 v37, v37
v_mul_f32_e32 v40, v40, v41
v_mul_f32_e32 v0, v0, v40
v_mul_f32_e32 v40, v78, v40
v_cvt_pk_bf16_f32 v0, v0, v40
s_nop 1
v_mov_b32_dpp v2, v0 quad_perm:[0,1,2,3] row_mask:0xf bank_mask:0x1
v_mov_b32_dpp v3, v0 row_ror:12 row_mask:0xf bank_mask:0x1
v_mov_b32_dpp v4, v0 row_ror:8 row_mask:0xf bank_mask:0x1
v_mov_b32_dpp v5, v0 row_ror:4 row_mask:0xf bank_mask:0x1
v_mov_b32_dpp v14, v0 row_ror:4 row_mask:0xf bank_mask:0x2
v_mov_b32_dpp v15, v0 quad_perm:[0,1,2,3] row_mask:0xf bank_mask:0x2
v_mov_b32_dpp v16, v0 row_ror:12 row_mask:0xf bank_mask:0x2
v_mov_b32_dpp v17, v0 row_ror:8 row_mask:0xf bank_mask:0x2
v_mov_b32_dpp v10, v0 row_ror:8 row_mask:0xf bank_mask:0x4
v_mov_b32_dpp v11, v0 row_ror:4 row_mask:0xf bank_mask:0x4
v_mov_b32_dpp v12, v0 quad_perm:[0,1,2,3] row_mask:0xf bank_mask:0x4
v_mov_b32_dpp v13, v0 row_ror:12 row_mask:0xf bank_mask:0x4
v_mov_b32_dpp v6, v0 row_ror:12 row_mask:0xf bank_mask:0x8
v_mov_b32_dpp v7, v0 row_ror:8 row_mask:0xf bank_mask:0x8
v_mov_b32_dpp v8, v0 row_ror:4 row_mask:0xf bank_mask:0x8
v_mov_b32_dpp v9, v0 quad_perm:[0,1,2,3] row_mask:0xf bank_mask:0x8
v_add_f32_e32 v0, v36, v37
s_nop 0
v_readlane_b32 s0, v0, s4
s_nop 1
v_perm_b32 v1, s0, v1, v136
s_mov_b32 s0, 0x5040100
)ASM"
R"ASM(
s_mov_b32 s1, 0x7060302
s_waitcnt vmcnt(2)
v_perm_b32 v64, v22, v18, s0
v_perm_b32 v36, v22, v18, s1
v_perm_b32 v48, v23, v19, s0
v_perm_b32 v40, v23, v19, s1
v_perm_b32 v52, v24, v20, s0
v_perm_b32 v44, v24, v20, s1
v_perm_b32 v60, v25, v21, s0
v_perm_b32 v56, v25, v21, s1
s_waitcnt vmcnt(0)
v_perm_b32 v65, v30, v26, s0
v_perm_b32 v49, v31, v27, s0
v_perm_b32 v53, v32, v28, s0
v_perm_b32 v61, v33, v29, s0
v_perm_b32 v37, v30, v26, s1
v_mfma_f32_16x16x32_bf16 v[18:21], v[62:65], v[2:5], 0
v_perm_b32 v41, v31, v27, s1
v_perm_b32 v45, v32, v28, s1
v_mfma_f32_16x16x32_bf16 v[18:21], v[46:49], v[14:17], v[18:21]
v_perm_b32 v57, v33, v29, s1
v_mfma_f32_16x16x32_bf16 v[18:21], v[50:53], v[10:13], v[18:21]
v_mfma_f32_16x16x32_bf16 v[18:21], v[58:61], v[6:9], v[18:21]
v_mfma_f32_16x16x32_bf16 v[2:5], v[34:37], v[2:5], 0
v_mfma_f32_16x16x32_bf16 v[2:5], v[38:41], v[14:17], v[2:5]
v_mfma_f32_16x16x32_bf16 v[2:5], v[42:45], v[10:13], v[2:5]
v_mfma_f32_16x16x32_bf16 v[2:5], v[54:57], v[6:9], v[2:5]
s_nop 3
ds_write_b128 v133, v[18:21]
s_nop 2
ds_write_b128 v133, v[2:5] offset:1024
ds_write_b32 v134, v135 offset:1248
s_mov_b32 s2, 0x1000000
s_branch .La_8
.La_7:
s_andn2_b64 vcc, exec, s[0:1]
s_cbranch_vccz .La_10
.La_8:
ds_read_b128 v[2:5], v132 offset:1248
ds_read2st64_b64 v[6:9], v131 offset1:4
ds_read2st64_b64 v[10:13], v131 offset0:8 offset1:12
s_waitcnt lgkmcnt(2)
v_and_b32_e32 v0, v2, v3
v_bitop3_b32 v0, v0, v5, v4 bitop3:0x80
s_waitcnt lgkmcnt(1)
v_pk_add_f32 v[2:3], v[6:7], v[8:9]
s_waitcnt lgkmcnt(0)
v_pk_add_f32 v[6:7], v[10:11], v[12:13]
v_readfirstlane_b32 s0, v0
v_pk_add_f32 v[2:3], v[2:3], v[6:7]
s_cmp_lg_u32 s0, 0
s_mov_b64 s[0:1], -1
s_cbranch_scc1 .La_7
s_setprio 0
s_add_i32 s2, s2, -1
s_cmp_eq_u32 s2, 0
s_cselect_b64 s[0:1], -1, 0
s_branch .La_7
.La_10:
v_cvt_pk_bf16_f32 v0, v2, v3
buffer_store_dwordx2 v[0:1], v130, s[20:23], 0 offen sc1
s_endpgm
.Lk_q2:
s_setprio 3
v_lshrrev_b32_e32 v88, 3, v0
v_and_b32_e32 v88, 6, v88
v_and_b32_e32 v81, 8, v0
v_or3_b32 v80, s19, v88, v80
v_mov_b32_e32 v88, 0x1000
v_lshl_add_u32 v80, v80, 2, v88
v_mov_b32_e32 v88, 0x7ffffff0
v_cmp_eq_u32_e32 vcc, 0, v81
s_cmp_lt_u32 s16, 8
s_cselect_b64 s[6:7], -1, 0
v_cndmask_b32_e32 v89, v88, v80, vcc
v_cmp_gt_u32_e32 vcc, 16, v1
v_and_b32_e32 v1, 0x78, v82
v_lshl_or_b32 v1, s17, 7, v1
v_or_b32_e32 v1, 0x400, v1
s_and_b64 vcc, s[6:7], vcc
v_cndmask_b32_e32 v80, v88, v1, vcc
v_lshl_or_b32 v1, s17, 13, v2
v_or_b32_e32 v81, 0x1000, v1
v_bfe_u32 v0, v0, 5, 1
v_and_b32_e32 v1, 0xf8, v82
s_movk_i32 s6, 0x110
v_mad_u32_u24 v82, v0, s6, v1
s_waitcnt vmcnt(20)
v_readfirstlane_b32 s6, v84
s_lshl_b32 s6, s6, 16
v_lshlrev_b32_e32 v0, 16, v84
v_and_b32_e32 v1, 0xffff0000, v84
v_subrev_f32_e32 v0, s6, v0
v_subrev_f32_e32 v1, s6, v1
v_add_f32_e32 v84, v0, v1
v_fma_f32 v1, v1, v1, 0
v_fmac_f32_e32 v1, v0, v0
v_lshlrev_b32_e32 v0, 16, v85
v_and_b32_e32 v85, 0xffff0000, v85
v_subrev_f32_e32 v0, s6, v0
v_subrev_f32_e32 v85, s6, v85
v_add_f32_e32 v88, v0, v85
v_fmac_f32_e32 v1, v85, v85
v_and_b32_e32 v85, 0xffff0000, v86
v_fmac_f32_e32 v1, v0, v0
v_lshlrev_b32_e32 v0, 16, v86
v_subrev_f32_e32 v85, s6, v85
v_subrev_f32_e32 v0, s6, v0
v_fmac_f32_e32 v1, v85, v85
v_add_f32_e32 v84, 0, v84
v_add_f32_e32 v86, v0, v85
v_fmac_f32_e32 v1, v0, v0
v_lshlrev_b32_e32 v0, 16, v87
v_and_b32_e32 v85, 0xffff0000, v87
v_add_f32_e32 v84, v88, v84
v_subrev_f32_e32 v0, s6, v0
v_subrev_f32_e32 v85, s6, v85
v_add_f32_e32 v84, v86, v84
v_add_f32_e32 v86, v0, v85
v_fmac_f32_e32 v1, v85, v85
v_add_f32_e32 v84, v86, v84
v_fmac_f32_e32 v1, v0, v0
s_movk_i32 s5, 0x1000
v_add_f32_dpp v0, v84, v84 row_ror:8 row_mask:0xf bank_mask:0xf bound_ctrl:1
v_add_f32_dpp v1, v1, v1 row_ror:8 row_mask:0xf bank_mask:0xf bound_ctrl:1
s_nop 0
v_add_f32_dpp v0, v0, v0 row_ror:4 row_mask:0xf bank_mask:0xf bound_ctrl:1
v_add_f32_dpp v1, v1, v1 row_ror:4 row_mask:0xf bank_mask:0xf bound_ctrl:1
s_nop 0
v_add_f32_dpp v0, v0, v0 row_ror:2 row_mask:0xf bank_mask:0xf bound_ctrl:1
v_add_f32_dpp v1, v1, v1 row_ror:2 row_mask:0xf bank_mask:0xf bound_ctrl:1
s_nop 0
v_add_f32_dpp v0, v0, v0 row_ror:1 row_mask:0xf bank_mask:0xf bound_ctrl:1
v_add_f32_dpp v1, v1, v1 row_ror:1 row_mask:0xf bank_mask:0xf bound_ctrl:1
v_mov_b32_e32 v84, v0
v_mov_b32_e32 v85, v1
s_nop 0
v_permlane16_swap_b32_e32 v0, v84
v_permlane16_swap_b32_e32 v1, v85
v_add_f32_e32 v0, v0, v84
v_add_f32_e32 v1, v1, v85
v_mov_b32_e32 v84, v0
v_mov_b32_e32 v85, v1
s_nop 0
v_permlane32_swap_b32_e32 v0, v84
v_permlane32_swap_b32_e32 v1, v85
v_add_f32_e32 v0, v0, v84
v_add_f32_e32 v1, v1, v85
v_mul_f32_e32 v84, 0x3b000000, v0
v_mul_f32_e32 v1, 0x3b000000, v1
v_fma_f32 v1, -v84, v84, v1
v_max_f32_e32 v1, 0, v1
v_cvt_pk_bf16_f32 v1, v1, v1
v_lshlrev_b32_e32 v1, 16, v1
v_add_f32_e32 v1, 0x3727c5ac, v1
v_cvt_pk_bf16_f32 v1, v1, v1
v_lshlrev_b32_e32 v1, 16, v1
v_sqrt_f32_e32 v1, v1
v_mov_b32_e32 v84, s6
v_fmac_f32_e32 v84, 0x3b000000, v0
v_cvt_pk_bf16_f32 v0, v84, v84
v_cvt_pk_bf16_f32 v1, v1, v1
v_lshlrev_b32_e32 v1, 16, v1
v_lshlrev_b32_e32 v0, 16, v0
v_rcp_f32_e32 v84, v1
s_waitcnt vmcnt(19)
v_lshlrev_b32_e32 v1, 16, v76
v_and_b32_e32 v76, 0xffff0000, v76
v_sub_f32_e32 v1, v1, v0
v_sub_f32_e32 v76, v76, v0
v_cvt_pk_bf16_f32 v1, v1, v76
v_lshlrev_b32_e32 v76, 16, v1
v_and_b32_e32 v1, 0xffff0000, v1
v_mul_f32_e32 v76, v84, v76
v_mul_f32_e32 v1, v84, v1
v_cvt_pk_bf16_f32 v1, v76, v1
s_waitcnt vmcnt(18)
v_lshlrev_b32_e32 v76, 16, v72
v_and_b32_e32 v72, 0xffff0000, v72
v_sub_f32_e32 v76, v76, v0
v_sub_f32_e32 v72, v72, v0
v_cvt_pk_bf16_f32 v72, v76, v72
v_lshlrev_b32_e32 v76, 16, v72
v_and_b32_e32 v72, 0xffff0000, v72
v_mul_f32_e32 v76, v84, v76
v_mul_f32_e32 v72, v84, v72
v_cvt_pk_bf16_f32 v72, v76, v72
v_lshlrev_b32_e32 v76, 16, v77
v_and_b32_e32 v77, 0xffff0000, v77
v_sub_f32_e32 v76, v76, v0
v_sub_f32_e32 v77, v77, v0
v_cvt_pk_bf16_f32 v76, v76, v77
v_lshlrev_b32_e32 v77, 16, v76
v_and_b32_e32 v76, 0xffff0000, v76
v_mul_f32_e32 v77, v84, v77
v_mul_f32_e32 v76, v84, v76
v_cvt_pk_bf16_f32 v76, v77, v76
v_lshlrev_b32_e32 v77, 16, v73
v_and_b32_e32 v73, 0xffff0000, v73
v_sub_f32_e32 v77, v77, v0
)ASM"
R"ASM(
v_sub_f32_e32 v73, v73, v0
v_cvt_pk_bf16_f32 v73, v77, v73
v_lshlrev_b32_e32 v77, 16, v73
v_and_b32_e32 v73, 0xffff0000, v73
v_mul_f32_e32 v77, v84, v77
v_mul_f32_e32 v73, v84, v73
v_cvt_pk_bf16_f32 v73, v77, v73
v_lshlrev_b32_e32 v77, 16, v78
v_and_b32_e32 v78, 0xffff0000, v78
v_sub_f32_e32 v77, v77, v0
v_sub_f32_e32 v78, v78, v0
v_cvt_pk_bf16_f32 v77, v77, v78
v_lshlrev_b32_e32 v78, 16, v77
v_and_b32_e32 v77, 0xffff0000, v77
v_mul_f32_e32 v78, v84, v78
v_mul_f32_e32 v77, v84, v77
v_cvt_pk_bf16_f32 v77, v78, v77
v_lshlrev_b32_e32 v78, 16, v74
v_and_b32_e32 v74, 0xffff0000, v74
v_sub_f32_e32 v78, v78, v0
v_sub_f32_e32 v74, v74, v0
v_cvt_pk_bf16_f32 v74, v78, v74
v_lshlrev_b32_e32 v78, 16, v74
v_and_b32_e32 v74, 0xffff0000, v74
v_mul_f32_e32 v78, v84, v78
v_mul_f32_e32 v74, v84, v74
v_cvt_pk_bf16_f32 v74, v78, v74
v_lshlrev_b32_e32 v78, 16, v79
v_and_b32_e32 v79, 0xffff0000, v79
v_sub_f32_e32 v78, v78, v0
v_sub_f32_e32 v79, v79, v0
v_cvt_pk_bf16_f32 v78, v78, v79
v_lshlrev_b32_e32 v79, 16, v78
v_and_b32_e32 v78, 0xffff0000, v78
v_mul_f32_e32 v79, v84, v79
v_mul_f32_e32 v78, v84, v78
v_cvt_pk_bf16_f32 v78, v79, v78
v_lshlrev_b32_e32 v79, 16, v75
v_and_b32_e32 v75, 0xffff0000, v75
v_sub_f32_e32 v79, v79, v0
v_sub_f32_e32 v75, v75, v0
v_cvt_pk_bf16_f32 v75, v79, v75
v_lshlrev_b32_e32 v79, 16, v75
v_and_b32_e32 v75, 0xffff0000, v75
v_mul_f32_e32 v79, v84, v79
v_mul_f32_e32 v75, v84, v75
v_cvt_pk_bf16_f32 v75, v79, v75
s_mov_b32 s6, 0x5040100
s_mov_b32 s7, 0x7060302
s_waitcnt vmcnt(15)
v_perm_b32 v79, v68, v64, s6
v_mov_b32_e32 v85, 0
v_perm_b32 v64, v68, v64, s7
v_mov_b32_e32 v68, 0
v_dot2c_f32_bf16_e32 v85, v79, v1
v_dot2c_f32_bf16_e32 v68, v64, v1
v_perm_b32 v64, v69, v65, s6
v_mov_b32_e32 v79, 0
v_dot2c_f32_bf16_e32 v79, v64, v1
v_perm_b32 v64, v69, v65, s7
v_mov_b32_e32 v65, 0
v_dot2c_f32_bf16_e32 v65, v64, v1
v_perm_b32 v64, v70, v66, s6
v_mov_b32_e32 v69, 0
v_dot2c_f32_bf16_e32 v69, v64, v1
v_perm_b32 v64, v70, v66, s7
v_mov_b32_e32 v66, 0
v_dot2c_f32_bf16_e32 v66, v64, v1
v_perm_b32 v64, v71, v67, s6
v_mov_b32_e32 v70, 0
v_dot2c_f32_bf16_e32 v70, v64, v1
v_perm_b32 v64, v71, v67, s7
v_mov_b32_e32 v67, 0
v_dot2c_f32_bf16_e32 v67, v64, v1
s_waitcnt vmcnt(13)
v_perm_b32 v1, v60, v56, s6
v_dot2c_f32_bf16_e32 v85, v1, v76
v_perm_b32 v1, v60, v56, s7
v_dot2c_f32_bf16_e32 v68, v1, v76
v_perm_b32 v1, v61, v57, s6
v_dot2c_f32_bf16_e32 v79, v1, v76
v_perm_b32 v1, v61, v57, s7
v_dot2c_f32_bf16_e32 v65, v1, v76
v_perm_b32 v1, v62, v58, s6
v_dot2c_f32_bf16_e32 v69, v1, v76
v_perm_b32 v1, v62, v58, s7
v_dot2c_f32_bf16_e32 v66, v1, v76
v_perm_b32 v1, v63, v59, s6
v_dot2c_f32_bf16_e32 v70, v1, v76
v_perm_b32 v1, v63, v59, s7
v_dot2c_f32_bf16_e32 v67, v1, v76
s_waitcnt vmcnt(11)
v_perm_b32 v1, v52, v48, s6
v_dot2c_f32_bf16_e32 v85, v1, v77
v_perm_b32 v1, v52, v48, s7
v_dot2c_f32_bf16_e32 v68, v1, v77
v_perm_b32 v1, v53, v49, s6
v_dot2c_f32_bf16_e32 v79, v1, v77
v_perm_b32 v1, v53, v49, s7
v_dot2c_f32_bf16_e32 v65, v1, v77
v_perm_b32 v1, v54, v50, s6
v_dot2c_f32_bf16_e32 v69, v1, v77
v_perm_b32 v1, v54, v50, s7
v_dot2c_f32_bf16_e32 v66, v1, v77
v_perm_b32 v1, v55, v51, s6
v_dot2c_f32_bf16_e32 v70, v1, v77
v_perm_b32 v1, v55, v51, s7
v_dot2c_f32_bf16_e32 v67, v1, v77
s_waitcnt vmcnt(9)
v_perm_b32 v1, v44, v40, s6
v_dot2c_f32_bf16_e32 v85, v1, v78
v_perm_b32 v1, v44, v40, s7
v_dot2c_f32_bf16_e32 v68, v1, v78
v_perm_b32 v1, v45, v41, s6
v_dot2c_f32_bf16_e32 v79, v1, v78
v_perm_b32 v1, v45, v41, s7
v_dot2c_f32_bf16_e32 v65, v1, v78
v_perm_b32 v1, v46, v42, s6
v_dot2c_f32_bf16_e32 v69, v1, v78
v_perm_b32 v1, v46, v42, s7
v_dot2c_f32_bf16_e32 v66, v1, v78
v_perm_b32 v1, v47, v43, s6
v_dot2c_f32_bf16_e32 v70, v1, v78
v_perm_b32 v1, v47, v43, s7
v_dot2c_f32_bf16_e32 v67, v1, v78
s_waitcnt vmcnt(7)
v_perm_b32 v1, v36, v32, s6
v_dot2c_f32_bf16_e32 v85, v1, v72
v_perm_b32 v1, v36, v32, s7
v_dot2c_f32_bf16_e32 v68, v1, v72
v_perm_b32 v1, v37, v33, s6
v_dot2c_f32_bf16_e32 v79, v1, v72
v_perm_b32 v1, v37, v33, s7
v_dot2c_f32_bf16_e32 v65, v1, v72
v_perm_b32 v1, v38, v34, s6
v_dot2c_f32_bf16_e32 v69, v1, v72
v_perm_b32 v1, v38, v34, s7
v_dot2c_f32_bf16_e32 v66, v1, v72
v_perm_b32 v1, v39, v35, s6
v_dot2c_f32_bf16_e32 v70, v1, v72
v_perm_b32 v1, v39, v35, s7
v_dot2c_f32_bf16_e32 v67, v1, v72
s_waitcnt vmcnt(5)
v_perm_b32 v1, v28, v24, s6
v_dot2c_f32_bf16_e32 v85, v1, v73
v_perm_b32 v1, v28, v24, s7
v_dot2c_f32_bf16_e32 v68, v1, v73
v_perm_b32 v1, v29, v25, s6
v_dot2c_f32_bf16_e32 v79, v1, v73
v_perm_b32 v1, v29, v25, s7
v_dot2c_f32_bf16_e32 v65, v1, v73
v_perm_b32 v1, v30, v26, s6
v_dot2c_f32_bf16_e32 v69, v1, v73
v_perm_b32 v1, v30, v26, s7
v_dot2c_f32_bf16_e32 v66, v1, v73
v_perm_b32 v1, v31, v27, s6
v_dot2c_f32_bf16_e32 v70, v1, v73
v_perm_b32 v1, v31, v27, s7
v_dot2c_f32_bf16_e32 v67, v1, v73
s_waitcnt vmcnt(3)
v_perm_b32 v1, v20, v16, s6
v_dot2c_f32_bf16_e32 v85, v1, v74
v_perm_b32 v1, v20, v16, s7
v_dot2c_f32_bf16_e32 v68, v1, v74
v_perm_b32 v1, v21, v17, s6
v_dot2c_f32_bf16_e32 v79, v1, v74
v_perm_b32 v1, v21, v17, s7
v_dot2c_f32_bf16_e32 v65, v1, v74
v_perm_b32 v1, v22, v18, s6
v_dot2c_f32_bf16_e32 v69, v1, v74
v_perm_b32 v1, v22, v18, s7
v_dot2c_f32_bf16_e32 v66, v1, v74
v_perm_b32 v1, v23, v19, s6
v_dot2c_f32_bf16_e32 v70, v1, v74
v_perm_b32 v1, v23, v19, s7
v_dot2c_f32_bf16_e32 v67, v1, v74
s_waitcnt vmcnt(1)
v_perm_b32 v1, v12, v8, s6
v_dot2c_f32_bf16_e32 v85, v1, v75
v_perm_b32 v1, v12, v8, s7
v_dot2c_f32_bf16_e32 v68, v1, v75
v_perm_b32 v1, v13, v9, s6
v_dot2c_f32_bf16_e32 v79, v1, v75
v_perm_b32 v1, v13, v9, s7
v_dot2c_f32_bf16_e32 v65, v1, v75
v_perm_b32 v1, v14, v10, s6
v_dot2c_f32_bf16_e32 v69, v1, v75
v_perm_b32 v1, v14, v10, s7
v_dot2c_f32_bf16_e32 v66, v1, v75
v_perm_b32 v1, v15, v11, s6
v_dot2c_f32_bf16_e32 v70, v1, v75
v_perm_b32 v1, v15, v11, s7
v_dot2c_f32_bf16_e32 v67, v1, v75
v_permlane32_swap_b32_e32 v85, v69
v_permlane32_swap_b32_e32 v79, v70
v_add_f32_e32 v1, v85, v69
v_permlane32_swap_b32_e32 v68, v66
v_add_f32_e32 v9, v79, v70
v_permlane32_swap_b32_e32 v65, v67
v_add_f32_e32 v8, v68, v66
)ASM"
R"ASM(
v_add_f32_e32 v10, v65, v67
v_permlane16_swap_b32_e32 v1, v9
v_add_f32_e32 v1, v1, v9
v_permlane16_swap_b32_e32 v8, v10
v_add_f32_e32 v8, v8, v10
s_nop 0
v_add_f32_dpp v10, v1, v1 row_ror:8 row_mask:0xf bank_mask:0xf bound_ctrl:1
v_add_u32_e32 v1, 1, v83
v_add_f32_dpp v11, v8, v8 row_ror:8 row_mask:0xf bank_mask:0xf bound_ctrl:1
v_lshrrev_b32_e32 v12, 8, v1
s_movk_i32 s6, 0xff00
s_movk_i32 s8, 0xff
v_and_b32_e32 v8, 0xff, v1
v_bfe_u32 v9, v1, 8, 8
v_bfi_b32 v10, s6, v10, v1
v_bfi_b32 v11, s6, v11, v12
buffer_store_dwordx2 v[10:11], v89, s[0:3], 0 offen sc1
s_waitcnt vmcnt(1)
v_lshlrev_b32_e32 v10, 16, v4
v_and_b32_e32 v4, 0xffff0000, v4
v_sub_f32_e32 v10, v10, v0
v_sub_f32_e32 v4, v4, v0
v_cvt_pk_bf16_f32 v4, v10, v4
v_lshlrev_b32_e32 v10, 16, v4
v_and_b32_e32 v4, 0xffff0000, v4
v_mul_f32_e32 v10, v84, v10
v_mul_f32_e32 v4, v84, v4
v_cvt_pk_bf16_f32 v4, v10, v4
v_lshlrev_b32_e32 v10, 16, v5
v_and_b32_e32 v5, 0xffff0000, v5
v_sub_f32_e32 v10, v10, v0
v_sub_f32_e32 v5, v5, v0
v_cvt_pk_bf16_f32 v5, v10, v5
v_lshlrev_b32_e32 v10, 16, v5
v_and_b32_e32 v5, 0xffff0000, v5
v_mul_f32_e32 v10, v84, v10
v_mul_f32_e32 v5, v84, v5
v_cvt_pk_bf16_f32 v5, v10, v5
v_lshlrev_b32_e32 v10, 16, v6
v_and_b32_e32 v6, 0xffff0000, v6
v_sub_f32_e32 v10, v10, v0
v_sub_f32_e32 v6, v6, v0
v_cvt_pk_bf16_f32 v6, v10, v6
v_lshlrev_b32_e32 v10, 16, v6
v_and_b32_e32 v6, 0xffff0000, v6
v_mul_f32_e32 v10, v84, v10
v_mul_f32_e32 v6, v84, v6
v_cvt_pk_bf16_f32 v6, v10, v6
v_lshlrev_b32_e32 v10, 16, v7
v_and_b32_e32 v7, 0xffff0000, v7
v_sub_f32_e32 v10, v10, v0
v_sub_f32_e32 v0, v7, v0
v_cvt_pk_bf16_f32 v0, v10, v0
v_lshlrev_b32_e32 v7, 16, v0
v_and_b32_e32 v0, 0xffff0000, v0
v_mul_f32_e32 v7, v84, v7
v_mul_f32_e32 v0, v84, v0
v_cvt_pk_bf16_f32 v7, v7, v0
ds_write_b128 v2, v[4:7] offset:2432
v_mov_b32_e32 v0, 1
v_mov_b32_e32 v2, s4
ds_write_b128 v3, v[0:3] offset:1280
s_sleep 5
s_add_i32 s6, s4, 0xd0
s_branch 2f
1:
s_sleep 1
2:
s_memrealtime vcc
s_waitcnt lgkmcnt(0)
s_sub_i32 vcc_lo, vcc_lo, s6
s_cmp_lt_i32 vcc_lo, -8
s_cbranch_scc1 1b
s_cmp_lt_i32 vcc_lo, 0
s_cbranch_scc1 2b
s_mov_b32 s9, 0x100000
s_movk_i32 s10, 0x800
s_movk_i32 s11, 0xc00
s_movk_i32 s12, 0x1400
s_movk_i32 s13, 0x1800
s_movk_i32 s14, 0x1c00
s_branch .Lq_2
.Lq_1:
s_add_i32 s9, s9, -1
s_cmp_eq_u32 s9, 0
s_cselect_b64 s[6:7], -1, 0
s_andn2_b64 vcc, exec, s[6:7]
s_cbranch_vccz .Lq_4
.Lq_2:
buffer_load_dwordx4 v[2:5], v81, s[0:3], 0 offen sc1
buffer_load_dwordx4 v[10:13], v81, s[0:3], s22 offen sc1
buffer_load_dwordx4 v[14:17], v81, s[0:3], s10 offen sc1
buffer_load_dwordx4 v[18:21], v81, s[0:3], s5 offen sc1
buffer_load_dwordx4 v[22:25], v81, s[0:3], s11 offen sc1
buffer_load_dwordx4 v[26:29], v81, s[0:3], s12 offen sc1
buffer_load_dwordx4 v[30:33], v81, s[0:3], s13 offen sc1
buffer_load_dwordx4 v[34:37], v81, s[0:3], s14 offen sc1
s_waitcnt vmcnt(7)
v_xor_b32_e32 v2, v2, v8
v_xor_b32_e32 v3, v3, v9
v_xor_b32_e32 v4, v4, v8
v_xor_b32_e32 v5, v5, v9
s_waitcnt vmcnt(6)
v_xor_b32_e32 v7, v11, v9
v_or3_b32 v0, v3, v2, v5
v_xor_b32_e32 v6, v10, v8
v_xor_b32_e32 v11, v13, v9
v_or3_b32 v0, v0, v4, v7
v_xor_b32_e32 v10, v12, v8
v_or3_b32 v0, v0, v6, v11
s_waitcnt vmcnt(5)
v_xor_b32_e32 v13, v15, v9
v_xor_b32_e32 v12, v14, v8
v_xor_b32_e32 v14, v16, v8
v_xor_b32_e32 v15, v17, v9
v_or3_b32 v0, v0, v10, v13
v_or3_b32 v0, v0, v12, v15
v_pk_add_f32 v[4:5], v[4:5], v[14:15]
v_pk_add_f32 v[2:3], v[2:3], v[12:13]
s_waitcnt vmcnt(3)
v_xor_b32_e32 v13, v23, v9
v_xor_b32_e32 v12, v22, v8
v_xor_b32_e32 v16, v24, v8
v_xor_b32_e32 v17, v25, v9
v_or3_b32 v0, v0, v14, v13
v_or3_b32 v0, v0, v12, v17
v_pk_add_f32 v[10:11], v[10:11], v[16:17]
v_pk_add_f32 v[6:7], v[6:7], v[12:13]
v_xor_b32_e32 v13, v19, v9
v_xor_b32_e32 v12, v18, v8
v_xor_b32_e32 v14, v20, v8
v_xor_b32_e32 v15, v21, v9
v_or3_b32 v0, v0, v16, v13
v_or3_b32 v0, v0, v12, v15
v_pk_add_f32 v[2:3], v[2:3], v[12:13]
v_pk_add_f32 v[4:5], v[4:5], v[14:15]
s_waitcnt vmcnt(2)
v_xor_b32_e32 v13, v27, v9
v_xor_b32_e32 v12, v26, v8
v_xor_b32_e32 v16, v28, v8
v_xor_b32_e32 v17, v29, v9
v_or3_b32 v0, v0, v14, v13
v_or3_b32 v0, v0, v12, v17
v_pk_add_f32 v[6:7], v[6:7], v[12:13]
v_pk_add_f32 v[10:11], v[10:11], v[16:17]
s_waitcnt vmcnt(1)
v_xor_b32_e32 v13, v31, v9
v_xor_b32_e32 v12, v30, v8
v_xor_b32_e32 v14, v32, v8
v_xor_b32_e32 v15, v33, v9
v_or3_b32 v0, v0, v16, v13
v_or3_b32 v0, v0, v12, v15
v_pk_add_f32 v[4:5], v[4:5], v[14:15]
v_pk_add_f32 v[2:3], v[2:3], v[12:13]
s_waitcnt vmcnt(0)
v_xor_b32_e32 v13, v35, v9
v_xor_b32_e32 v12, v34, v8
v_xor_b32_e32 v16, v36, v8
v_xor_b32_e32 v17, v37, v9
v_or3_b32 v0, v0, v14, v13
v_or3_b32 v0, v0, v12, v17
v_pk_add_f32 v[10:11], v[10:11], v[16:17]
v_pk_add_f32 v[6:7], v[6:7], v[12:13]
v_bitop3_b32 v0, v0, s8, v16 bitop3:0xc8
v_cvt_pk_bf16_f32 v2, v2, v3
v_cvt_pk_bf16_f32 v3, v4, v5
v_cvt_pk_bf16_f32 v4, v6, v7
v_cvt_pk_bf16_f32 v5, v10, v11
v_cmp_ne_u32_e32 vcc, 0, v0
ds_write2_b64 v82, v[2:3], v[4:5] offset1:68
s_cbranch_vccnz .Lq_1
.Lq_4:
v_mov_b32_e32 v0, 1
v_mov_b32_e32 v2, 0
ds_write_b64 v2, v[0:1] offset:1216
v_mov_b32_e32 v0, s4
buffer_store_dwordx2 v[0:1], v80, s[0:3], 0 offen sc1
s_endpgm
.Lk_c:
s_and_b32 s17, s16, 7
s_waitcnt lgkmcnt(0)
s_mov_b64 s[20:21], s[72:73]
s_mov_b64 s[24:25], s[64:65]
s_cmp_gt_u32 s17, 3
s_mov_b32 s2, s64
s_mov_b32 s3, s65
s_mov_b32 s4, s66
s_mov_b32 s5, s67
s_mov_b32 s6, s68
s_mov_b32 s7, s69
s_mov_b32 s8, s70
s_mov_b32 s9, s71
s_mov_b32 s10, s72
s_mov_b32 s11, s73
s_mov_b32 s12, s74
s_mov_b32 s13, s75
s_mov_b32 s14, s76
s_mov_b32 s15, s77
s_cbranch_scc0 .Lc_2
s_mul_i32 s2, s17, 3
)ASM"
R"ASM(
s_add_i32 s2, s2, 4
s_cbranch_execz .Lc_3
s_branch .Lc_4
.Lc_2:
.Lc_3:
s_lshl_b32 s2, s17, 2
.Lc_4:
s_mov_b32 s3, 0
s_load_dwordx2 s[4:5], s[0:1], 0x40
s_load_dwordx2 s[8:9], s[0:1], 0x38
s_waitcnt lgkmcnt(0)
s_add_u32 s0, s14, 0x1bf200
s_addc_u32 s1, s15, 0
s_lshl_b32 s10, s17, 24
s_add_u32 s6, s6, s10
s_addc_u32 s7, s7, 0
s_add_u32 s6, s6, 0xf00000
s_addc_u32 s7, s7, 0
s_lshl_b64 s[2:3], s[2:3], 16
s_add_u32 s2, s8, s2
s_addc_u32 s3, s9, s3
s_lshl_b32 s8, s17, 19
s_add_u32 s8, s12, s8
s_addc_u32 s9, s13, 0
s_load_dword s10, s[14:15], 0
s_load_dword s10, s[0:1], 0
s_load_dword s10, s[6:7], 0
s_load_dword s10, s[2:3], 0
s_load_dword s10, s[8:9], 0
s_waitcnt lgkmcnt(0)
s_mov_b32 s2, 0x1000000
v_mov_b32_e32 v1, 0
s_branch .Lc_6
.Lc_5:
s_andn2_b64 vcc, exec, s[0:1]
s_cbranch_vccz .Lc_8
.Lc_6:
ds_read_b32 v2, v1 offset:1280
s_waitcnt lgkmcnt(0)
v_readfirstlane_b32 s0, v2
s_cmp_lg_u32 s0, 0
s_mov_b64 s[0:1], -1
s_cbranch_scc1 .Lc_5
s_add_i32 s2, s2, -1
s_cmp_eq_u32 s2, 0
s_cselect_b64 s[0:1], -1, 0
s_sleep 1
s_branch .Lc_5
.Lc_8:
s_ashr_i32 s1, s16, 3
s_lshl_b32 s0, s16, 3
s_and_b32 s0, s0, 8
s_and_b32 s2, s1, 7
s_or_b32 s8, s0, s2
s_lshl_b32 s0, s17, 6
s_lshl_b32 s2, s16, 6
s_and_b32 s0, s0, 0x100
s_and_b32 s2, s2, 0x80
s_or_b32 s0, s2, s0
s_lshl_b32 s2, s8, 3
s_or_b32 s0, s0, s2
s_lshr_b32 s2, s16, 4
s_and_b32 s2, s2, 4
v_bfe_u32 v1, v0, 4, 2
s_or_b32 s0, s0, s2
v_or_b32_e32 v17, s0, v1
s_and_b32 s25, s25, 0xffff
s_mov_b32 s27, 0x20000
s_movk_i32 s26, 0x400
v_lshlrev_b32_e32 v2, 1, v17
buffer_load_ushort v18, v2, s[24:27], 0 offen
s_ashr_i32 s2, s16, 4
s_lshl_b32 s3, s17, 4
s_lshr_b32 s6, s16, 2
s_mov_b32 s0, 0
v_mov_b32_e32 v5, 0x504
s_and_b32 s10, s1, 1
s_add_i32 s11, s3, s2
s_and_b32 s14, s3, 0x60
s_and_b32 s15, s6, 16
s_mov_b32 s2, s0
s_mov_b32 s3, s0
ds_read2_b32 v[128:129], v5 offset1:1
s_mov_b32 s1, s0
s_or_b32 s14, s15, s14
s_lshl_b32 s15, s11, 11
s_lshl_b32 s17, s10, 10
v_mov_b64_e32 v[14:15], s[2:3]
v_lshlrev_b32_e32 v1, 1, v1
v_mov_b64_e32 v[12:13], s[0:1]
s_or_b32 s0, s14, s8
s_or_b32 s1, s15, s17
v_and_b32_e32 v136, 63, v0
v_and_b32_e32 v2, 15, v0
v_lshrrev_b32_e32 v3, 1, v0
v_lshlrev_b32_e32 v4, 7, v0
s_lshl_b32 s11, s11, 5
v_and_or_b32 v0, v0, 1, v1
s_lshl_b32 s10, s10, 9
s_add_i32 s1, s1, 0xa1000
s_lshl_b32 s0, s0, 4
v_lshlrev_b32_e32 v20, 4, v136
v_and_b32_e32 v4, 0x380, v4
v_and_or_b32 v23, v3, 7, s11
v_lshlrev_b32_e32 v24, 4, v0
v_or_b32_e32 v25, s11, v1
s_cmpk_lt_i32 s16, 0x81
v_mov_b32_e32 v19, 0x7ffffff0
s_mov_b32 s9, 0x1000100
v_lshlrev_b32_e32 v21, 4, v2
v_cmp_eq_u32_e32 vcc, 0, v2
v_or_b32_e32 v22, 0x400, v4
v_add_u32_e32 v140, 0x580, v20
v_mov_b64_e32 v[8:9], v[12:13]
v_mov_b64_e32 v[4:5], v[12:13]
v_mov_b64_e32 v[0:1], v[12:13]
v_lshl_or_b32 v23, v23, 7, v24
v_lshlrev_b32_e32 v24, 10, v25
v_or_b32_e32 v139, s1, v20
v_lshl_or_b32 v20, v136, 11, s0
s_cselect_b64 s[0:1], -1, 0
s_and_b32 s5, s5, 0xffff
v_mov_b32_e32 v137, 0
v_mov_b64_e32 v[10:11], v[14:15]
v_mov_b64_e32 v[6:7], v[14:15]
v_mov_b64_e32 v[2:3], v[14:15]
v_add_u32_e32 v141, 0x21000, v23
v_or3_b32 v32, v24, s10, v21
v_add_u32_e32 v133, 0xa1000, v20
s_waitcnt lgkmcnt(0)
v_and_b32_e32 v134, 0xff, v128
v_bfe_u32 v135, v128, 8, 8
v_perm_b32 v142, v128, v128, s9
v_cndmask_b32_e64 v138, v19, v22, s[0:1]
s_cmpk_lt_i32 s16, 0x80
s_cselect_b64 s[8:9], -1, 0
s_and_b64 vcc, s[8:9], vcc
v_mov_b32_e32 v16, 0
s_mov_b32 s6, s26
s_mov_b32 s7, s27
v_readfirstlane_b32 s10, v129
s_waitcnt vmcnt(0)
v_lshlrev_b32_e32 v132, 16, v18
s_nop 0
v_lshlrev_b32_e32 v17, 1, v17
v_cndmask_b32_e32 v129, v19, v17, vcc
s_setprio 3
s_mov_b32 s11, 0x1000000
s_branch .Lc_10
.Lc_9:
s_andn2_b64 vcc, exec, s[2:3]
s_cbranch_vccz .Lc_12
.Lc_10:
ds_read_b128 v[18:21], v16 offset:1264
s_waitcnt lgkmcnt(0)
v_and_b32_e32 v17, v18, v19
v_and_b32_e32 v17, v17, v20
v_and_b32_e32 v17, v17, v21
s_nop 0
v_readfirstlane_b32 s2, v17
s_cmp_lg_u32 s2, 0
s_mov_b64 s[2:3], -1
s_cbranch_scc1 .Lc_9
s_add_i32 s11, s11, -1
s_cmp_eq_u32 s11, 0
s_cselect_b64 s[2:3], -1, 0
s_sleep 1
s_branch .Lc_9
.Lc_12:
s_and_b32 s13, s13, 0xffff
s_mov_b32 s15, 0x20000
s_mov_b32 s14, 0x400000
s_memrealtime s[2:3]
s_waitcnt lgkmcnt(0)
s_sub_i32 s2, s2, s10
s_max_i32 s2, s2, 0x136
s_add_i32 s2, s10, s2
s_add_i32 s3, s2, 36
s_branch 2f
1:
s_sleep 1
2:
s_memrealtime vcc
s_waitcnt lgkmcnt(0)
s_sub_i32 vcc_lo, vcc_lo, s3
s_cmp_lt_i32 vcc_lo, -8
s_cbranch_scc1 1b
s_cmp_lt_i32 vcc_lo, 0
s_cbranch_scc1 2b
buffer_load_dwordx4 v[24:27], v32, s[12:15], 0 offen nt
buffer_load_dwordx4 v[16:19], v32, s[12:15], 0 offen offset:256 nt
s_movk_i32 s3, 0x2000
buffer_load_dwordx4 v[28:31], v32, s[12:15], s3 offen nt
buffer_load_dwordx4 v[20:23], v32, s[12:15], s3 offen offset:256 nt
s_add_i32 s3, s2, 49
s_branch 2f
1:
s_sleep 1
2:
s_memrealtime vcc
)ASM"
R"ASM(
s_waitcnt lgkmcnt(0)
s_sub_i32 vcc_lo, vcc_lo, s3
s_cmp_lt_i32 vcc_lo, -8
s_cbranch_scc1 1b
s_cmp_lt_i32 vcc_lo, 0
s_cbranch_scc1 2b
s_movk_i32 s3, 0x4000
buffer_load_dwordx4 v[116:119], v32, s[12:15], s3 offen nt
buffer_load_dwordx4 v[100:103], v32, s[12:15], s3 offen offset:256 nt
s_movk_i32 s3, 0x6000
buffer_load_dwordx4 v[124:127], v32, s[12:15], s3 offen nt
buffer_load_dwordx4 v[112:115], v32, s[12:15], s3 offen offset:256 nt
s_add_i32 s3, s2, 62
s_branch 2f
1:
s_sleep 1
2:
s_memrealtime vcc
s_waitcnt lgkmcnt(0)
s_sub_i32 vcc_lo, vcc_lo, s3
s_cmp_lt_i32 vcc_lo, -8
s_cbranch_scc1 1b
s_cmp_lt_i32 vcc_lo, 0
s_cbranch_scc1 2b
s_movk_i32 s3, 0x400
buffer_load_dwordx4 v[108:111], v32, s[12:15], s3 offen nt
buffer_load_dwordx4 v[96:99], v32, s[12:15], s3 offen offset:256 nt
s_movk_i32 s3, 0x2400
buffer_load_dwordx4 v[120:123], v32, s[12:15], s3 offen nt
buffer_load_dwordx4 v[104:107], v32, s[12:15], s3 offen offset:256 nt
s_addk_i32 s2, 0x4b
s_branch 2f
1:
s_sleep 1
2:
s_memrealtime vcc
s_waitcnt lgkmcnt(0)
s_sub_i32 vcc_lo, vcc_lo, s2
s_cmp_lt_i32 vcc_lo, -8
s_cbranch_scc1 1b
s_cmp_lt_i32 vcc_lo, 0
s_cbranch_scc1 2b
s_movk_i32 s2, 0x4400
buffer_load_dwordx4 v[88:91], v32, s[12:15], s2 offen nt
buffer_load_dwordx4 v[80:83], v32, s[12:15], s2 offen offset:256 nt
s_movk_i32 s2, 0x6400
buffer_load_dwordx4 v[92:95], v32, s[12:15], s2 offen nt
buffer_load_dwordx4 v[84:87], v32, s[12:15], s2 offen offset:256 nt
s_mov_b32 s11, 0x1000000
v_mov_b32_e32 v32, 0
s_branch .Lc_14
.Lc_13:
s_andn2_b64 vcc, exec, s[2:3]
s_cbranch_vccz .Lc_16
.Lc_14:
ds_read_b128 v[34:37], v32 offset:1248
s_waitcnt lgkmcnt(0)
v_and_b32_e32 v33, v34, v35
v_and_b32_e32 v33, v33, v36
v_and_b32_e32 v33, v33, v37
s_nop 0
v_readfirstlane_b32 s2, v33
s_cmp_lg_u32 s2, 0
s_mov_b64 s[2:3], -1
s_cbranch_scc1 .Lc_13
s_add_i32 s11, s11, -1
s_cmp_eq_u32 s11, 0
s_cselect_b64 s[2:3], -1, 0
s_sleep 1
s_branch .Lc_13
.Lc_16:
s_mov_b32 s23, 0x20000
s_mov_b32 s22, 0xe1000
s_and_b32 s21, s21, 0xffff
s_sleep 13
s_add_i32 s2, s10, 0x1cc
s_branch 2f
1:
s_sleep 1
2:
s_memrealtime vcc
s_waitcnt lgkmcnt(0)
s_sub_i32 vcc_lo, vcc_lo, s2
s_cmp_lt_i32 vcc_lo, -8
s_cbranch_scc1 1b
s_cmp_lt_i32 vcc_lo, 0
s_cbranch_scc1 2b
s_movk_i32 s12, 0x800
s_movk_i32 s11, 0x400
buffer_load_dwordx4 v[144:147], v141, s[20:23], 0 offen sc1
buffer_load_dwordx4 v[148:151], v141, s[20:23], s11 offen sc1
s_movk_i32 s13, 0xc00
buffer_load_dwordx4 v[152:155], v141, s[20:23], s12 offen sc1
buffer_load_dwordx4 v[156:159], v141, s[20:23], s13 offen sc1
buffer_load_dwordx2 v[130:131], v138, s[20:23], 0 offen sc1
s_mov_b32 s14, 0x5040100
s_mov_b32 s15, 0x7060302
s_waitcnt vmcnt(18)
v_perm_b32 v76, v28, v24, s14
v_perm_b32 v72, v28, v24, s15
v_perm_b32 v68, v29, v25, s14
v_perm_b32 v64, v29, v25, s15
v_perm_b32 v60, v30, v26, s14
v_perm_b32 v56, v30, v26, s15
v_perm_b32 v52, v31, v27, s14
v_perm_b32 v48, v31, v27, s15
s_waitcnt vmcnt(17)
v_perm_b32 v44, v20, v16, s14
v_perm_b32 v40, v20, v16, s15
v_perm_b32 v36, v21, v17, s14
v_perm_b32 v32, v21, v17, s15
v_perm_b32 v28, v22, v18, s14
v_perm_b32 v24, v22, v18, s15
v_perm_b32 v20, v23, v19, s14
v_perm_b32 v16, v23, v19, s15
s_waitcnt vmcnt(14)
v_perm_b32 v77, v124, v116, s14
v_perm_b32 v73, v124, v116, s15
v_perm_b32 v69, v125, v117, s14
v_perm_b32 v65, v125, v117, s15
v_perm_b32 v61, v126, v118, s14
v_perm_b32 v57, v126, v118, s15
v_perm_b32 v53, v127, v119, s14
v_perm_b32 v49, v127, v119, s15
s_waitcnt vmcnt(13)
v_perm_b32 v45, v112, v100, s14
v_perm_b32 v41, v112, v100, s15
v_perm_b32 v37, v113, v101, s14
v_perm_b32 v33, v113, v101, s15
v_perm_b32 v29, v114, v102, s14
v_perm_b32 v25, v114, v102, s15
v_perm_b32 v21, v115, v103, s14
v_perm_b32 v17, v115, v103, s15
s_waitcnt vmcnt(10)
v_perm_b32 v78, v120, v108, s14
v_perm_b32 v74, v120, v108, s15
v_perm_b32 v70, v121, v109, s14
v_perm_b32 v66, v121, v109, s15
v_perm_b32 v62, v122, v110, s14
v_perm_b32 v58, v122, v110, s15
v_perm_b32 v54, v123, v111, s14
v_perm_b32 v50, v123, v111, s15
s_waitcnt vmcnt(9)
v_perm_b32 v46, v104, v96, s14
v_perm_b32 v42, v104, v96, s15
v_perm_b32 v38, v105, v97, s14
v_perm_b32 v34, v105, v97, s15
v_perm_b32 v30, v106, v98, s14
v_perm_b32 v26, v106, v98, s15
v_perm_b32 v22, v107, v99, s14
v_perm_b32 v18, v107, v99, s15
s_waitcnt vmcnt(6)
v_perm_b32 v79, v92, v88, s14
v_perm_b32 v75, v92, v88, s15
v_perm_b32 v71, v93, v89, s14
v_perm_b32 v67, v93, v89, s15
v_perm_b32 v63, v94, v90, s14
v_perm_b32 v59, v94, v90, s15
v_perm_b32 v55, v95, v91, s14
v_perm_b32 v51, v95, v91, s15
s_waitcnt vmcnt(5)
v_perm_b32 v47, v84, v80, s14
v_perm_b32 v43, v84, v80, s15
v_perm_b32 v39, v85, v81, s14
v_perm_b32 v35, v85, v81, s15
v_perm_b32 v31, v86, v82, s14
v_perm_b32 v27, v86, v82, s15
v_perm_b32 v23, v87, v83, s14
v_perm_b32 v19, v87, v83, s15
s_waitcnt vmcnt(4)
v_perm_b32 v80, v147, v145, s14
v_xor_b32_e32 v84, v80, v142
v_perm_b32 v96, v147, v145, s15
v_lshlrev_b32_e32 v82, 16, v144
v_and_b32_e32 v83, 0xffff0000, v144
v_lshlrev_b32_e32 v80, 16, v146
v_and_b32_e32 v81, 0xffff0000, v146
s_waitcnt vmcnt(3)
v_perm_b32 v85, v151, v149, s14
v_bitop3_b32 v88, v85, v84, v142 bitop3:0xde
v_perm_b32 v97, v151, v149, s15
v_lshlrev_b32_e32 v84, 16, v148
v_and_b32_e32 v85, 0xffff0000, v148
v_lshlrev_b32_e32 v86, 16, v150
v_and_b32_e32 v87, 0xffff0000, v150
s_waitcnt vmcnt(2)
v_perm_b32 v89, v155, v153, s14
v_bitop3_b32 v92, v89, v88, v142 bitop3:0xde
v_perm_b32 v99, v155, v153, s15
v_lshlrev_b32_e32 v88, 16, v152
v_and_b32_e32 v89, 0xffff0000, v152
v_lshlrev_b32_e32 v90, 16, v154
v_and_b32_e32 v91, 0xffff0000, v154
s_waitcnt vmcnt(1)
v_perm_b32 v93, v159, v157, s14
v_bitop3_b32 v98, v93, v92, v142 bitop3:0xde
v_perm_b32 v100, v159, v157, s15
v_lshlrev_b32_e32 v92, 16, v156
v_and_b32_e32 v93, 0xffff0000, v156
v_lshlrev_b32_e32 v94, 16, v158
)ASM"
R"ASM(
v_and_b32_e32 v95, 0xffff0000, v158
s_nop 0
v_cmp_ne_u32_e32 vcc, 0, v98
s_cbranch_vccnz .Lc_33
.Lc_17:
v_max_f32_e32 v98, v99, v100
v_max3_f32 v98, v96, v97, v98
s_nop 1
v_max_f32_dpp v98, v98, v98 row_ror:8 row_mask:0xf bank_mask:0xf bound_ctrl:1
s_nop 1
v_max_f32_dpp v98, v98, v98 row_ror:4 row_mask:0xf bank_mask:0xf bound_ctrl:1
s_nop 1
v_max_f32_dpp v101, v98, v98 row_ror:2 row_mask:0xf bank_mask:0xf bound_ctrl:1
v_sub_f32_e32 v96, v96, v101
v_exp_f32_e32 v96, v96
v_sub_f32_e32 v97, v97, v101
v_exp_f32_e32 v98, v97
v_sub_f32_e32 v97, v99, v101
v_exp_f32_e32 v102, v97
v_sub_f32_e32 v97, v100, v101
v_exp_f32_e32 v100, v97
v_pk_mul_f32 v[82:83], v[82:83], v[96:97] op_sel_hi:[1,0]
v_pk_mul_f32 v[80:81], v[80:81], v[96:97] op_sel_hi:[1,0]
v_pk_fma_f32 v[82:83], v[98:99], v[84:85], v[82:83] op_sel_hi:[0,1,1]
v_pk_fma_f32 v[80:81], v[98:99], v[86:87], v[80:81] op_sel_hi:[0,1,1]
v_pk_fma_f32 v[82:83], v[102:103], v[88:89], v[82:83] op_sel_hi:[0,1,1]
v_pk_fma_f32 v[80:81], v[102:103], v[90:91], v[80:81] op_sel_hi:[0,1,1]
v_pk_fma_f32 v[82:83], v[100:101], v[92:93], v[82:83] op_sel_hi:[0,1,1]
v_pk_fma_f32 v[80:81], v[100:101], v[94:95], v[80:81] op_sel_hi:[0,1,1]
s_nop 0
v_add_f32_dpp v82, v82, v82 row_ror:8 row_mask:0xf bank_mask:0xf bound_ctrl:1
v_add_f32_dpp v83, v83, v83 row_ror:8 row_mask:0xf bank_mask:0xf bound_ctrl:1
v_add_f32_dpp v80, v80, v80 row_ror:8 row_mask:0xf bank_mask:0xf bound_ctrl:1
v_add_f32_dpp v81, v81, v81 row_ror:8 row_mask:0xf bank_mask:0xf bound_ctrl:1
v_add_f32_dpp v82, v82, v82 row_ror:4 row_mask:0xf bank_mask:0xf bound_ctrl:1
v_add_f32_dpp v83, v83, v83 row_ror:4 row_mask:0xf bank_mask:0xf bound_ctrl:1
v_add_f32_dpp v80, v80, v80 row_ror:4 row_mask:0xf bank_mask:0xf bound_ctrl:1
v_add_f32_dpp v81, v81, v81 row_ror:4 row_mask:0xf bank_mask:0xf bound_ctrl:1
v_add_f32_dpp v82, v82, v82 row_ror:2 row_mask:0xf bank_mask:0xf bound_ctrl:1
v_add_f32_dpp v83, v83, v83 row_ror:2 row_mask:0xf bank_mask:0xf bound_ctrl:1
v_add_f32_dpp v80, v80, v80 row_ror:2 row_mask:0xf bank_mask:0xf bound_ctrl:1
v_add_f32_dpp v81, v81, v81 row_ror:2 row_mask:0xf bank_mask:0xf bound_ctrl:1
v_cvt_pk_bf16_f32 v82, v82, v83
v_cvt_pk_bf16_f32 v80, v80, v81
s_nop 0
v_mov_b32_dpp v12, v82 quad_perm:[0,1,2,3] row_mask:0xf bank_mask:0x1
v_mov_b32_dpp v13, v80 quad_perm:[0,1,2,3] row_mask:0xf bank_mask:0x1
v_mov_b32_dpp v14, v82 quad_perm:[1,0,3,2] row_mask:0xf bank_mask:0x1
v_mov_b32_dpp v15, v80 quad_perm:[1,0,3,2] row_mask:0xf bank_mask:0x1
v_mov_b32_dpp v8, v82 quad_perm:[0,1,2,3] row_mask:0xf bank_mask:0x2
v_mov_b32_dpp v9, v80 quad_perm:[0,1,2,3] row_mask:0xf bank_mask:0x2
v_mfma_f32_16x16x32_bf16 v[76:79], v[12:15], v[76:79], 0
v_mov_b32_dpp v10, v82 quad_perm:[1,0,3,2] row_mask:0xf bank_mask:0x2
v_mov_b32_dpp v11, v80 quad_perm:[1,0,3,2] row_mask:0xf bank_mask:0x2
v_mfma_f32_16x16x32_bf16 v[72:75], v[12:15], v[72:75], 0
v_mov_b32_dpp v4, v82 quad_perm:[0,1,2,3] row_mask:0xf bank_mask:0x4
v_mov_b32_dpp v5, v80 quad_perm:[0,1,2,3] row_mask:0xf bank_mask:0x4
v_mfma_f32_16x16x32_bf16 v[68:71], v[12:15], v[68:71], 0
v_mfma_f32_16x16x32_bf16 v[12:15], v[12:15], v[64:67], 0
v_mov_b32_dpp v6, v82 quad_perm:[1,0,3,2] row_mask:0xf bank_mask:0x4
v_mov_b32_dpp v7, v80 quad_perm:[1,0,3,2] row_mask:0xf bank_mask:0x4
v_mov_b32_dpp v0, v82 quad_perm:[0,1,2,3] row_mask:0xf bank_mask:0x8
v_mov_b32_dpp v1, v80 quad_perm:[0,1,2,3] row_mask:0xf bank_mask:0x8
v_mfma_f32_16x16x32_bf16 v[60:63], v[8:11], v[60:63], v[76:79]
v_mov_b32_dpp v2, v82 quad_perm:[1,0,3,2] row_mask:0xf bank_mask:0x8
v_mov_b32_dpp v3, v80 quad_perm:[1,0,3,2] row_mask:0xf bank_mask:0x8
v_mfma_f32_16x16x32_bf16 v[56:59], v[8:11], v[56:59], v[72:75]
v_add_f32_e32 v64, v98, v96
v_add_f32_e32 v65, v100, v102
v_mfma_f32_16x16x32_bf16 v[52:55], v[8:11], v[52:55], v[68:71]
v_mfma_f32_16x16x32_bf16 v[12:15], v[8:11], v[48:51], v[12:15]
v_add_f32_e32 v64, v65, v64
s_nop 1
v_add_f32_dpp v8, v64, v64 row_ror:8 row_mask:0xf bank_mask:0xf bound_ctrl:1
v_mfma_f32_16x16x32_bf16 v[44:47], v[4:7], v[44:47], v[60:63]
s_nop 0
v_add_f32_dpp v48, v8, v8 row_ror:4 row_mask:0xf bank_mask:0xf bound_ctrl:1
v_mfma_f32_16x16x32_bf16 v[40:43], v[4:7], v[40:43], v[56:59]
s_nop 2
ds_read_b32 v56, v137 offset:1296
ds_read_b128 v[8:11], v140
v_add_f32_dpp v57, v48, v48 row_ror:2 row_mask:0xf bank_mask:0xf bound_ctrl:1
v_mfma_f32_16x16x32_bf16 v[48:51], v[4:7], v[36:39], v[52:55]
v_rcp_f32_e32 v36, v57
v_mfma_f32_16x16x32_bf16 v[32:35], v[4:7], v[32:35], v[12:15]
v_mfma_f32_16x16x32_bf16 v[28:31], v[0:3], v[28:31], v[44:47]
v_mfma_f32_16x16x32_bf16 v[12:15], v[0:3], v[24:27], v[40:43]
v_mfma_f32_16x16x32_bf16 v[4:7], v[0:3], v[20:23], v[48:51]
v_mfma_f32_16x16x32_bf16 v[0:3], v[0:3], v[16:19], v[32:35]
s_waitcnt lgkmcnt(1)
v_readfirstlane_b32 s2, v56
s_cmp_lg_u32 s2, 0
s_cbranch_scc1 .Lc_20
s_nop 3
v_mov_b32_e32 v1, 0xffffff
.Lc_19:
ds_read_b32 v2, v137 offset:1296
v_subrev_co_u32_e32 v1, vcc, 1, v1
s_waitcnt lgkmcnt(1)
ds_read_b128 v[8:11], v140
s_waitcnt lgkmcnt(1)
v_readfirstlane_b32 s2, v2
s_cmp_lg_u32 s2, 0
s_cselect_b64 s[2:3], -1, 0
s_or_b64 s[2:3], s[2:3], vcc
s_andn2_b64 vcc, exec, s[2:3]
s_waitcnt lgkmcnt(0)
s_cbranch_vccnz .Lc_19
.Lc_20:
s_waitcnt lgkmcnt(0)
s_nop 2
v_fma_f32 v1, v28, v36, v8
s_movk_i32 s2, 0xff00
v_and_or_b32 v2, v1, s2, v134
v_fma_f32 v1, v12, v36, v9
v_and_or_b32 v3, v1, s2, v135
v_fma_f32 v1, v4, v36, v10
v_fmac_f32_e32 v11, v0, v36
v_and_or_b32 v4, v1, s2, v134
v_and_or_b32 v5, v11, s2, v135
buffer_store_dwordx4 v[2:5], v139, s[20:23], 0 offen sc1
s_andn2_b64 vcc, exec, s[0:1]
s_cbranch_vccnz .Lc_22
s_waitcnt vmcnt(1)
v_cmp_ne_u32_e32 vcc, v131, v128
s_cbranch_vccnz .Lc_35
.Lc_22:
s_cmpk_lg_i32 s16, 0x80
s_cbranch_scc0 .Lc_25
s_andn2_b64 vcc, exec, s[8:9]
s_cbranch_vccz .Lc_26
.Lc_24:
s_endpgm
.Lc_25:
v_mov_b32_e32 v0, 0x7ffffff0
v_cmp_eq_u32_e32 vcc, 0, v136
s_nop 1
v_cndmask_b32_e64 v0, v0, 0, vcc
buffer_store_dword v128, v0, s[20:23], 0 offen sc1
s_andn2_b64 vcc, exec, s[8:9]
s_cbranch_vccnz .Lc_24
.Lc_26:
s_waitcnt vmcnt(1)
v_subrev_u32_e32 v0, s10, v130
s_sleep 2
s_nop 0
v_max_i32_dpp v0, v0, v0 quad_perm:[1,0,3,2] row_mask:0xf bank_mask:0xf bound_ctrl:1
s_nop 1
v_max_i32_dpp v0, v0, v0 quad_perm:[2,3,0,1] row_mask:0xf bank_mask:0xf bound_ctrl:1
s_nop 1
v_max_i32_dpp v0, v0, v0 row_ror:4 row_mask:0xf bank_mask:0xf bound_ctrl:1
s_nop 0
v_readfirstlane_b32 s0, v0
s_min_i32 s0, s0, 0x3e8
s_add_i32 s0, s10, s0
s_addk_i32 s0, 0x214
s_branch 2f
1:
s_sleep 1
2:
s_memrealtime vcc
s_waitcnt lgkmcnt(0)
s_sub_i32 vcc_lo, vcc_lo, s0
s_cmp_lt_i32 vcc_lo, -8
s_cbranch_scc1 1b
s_cmp_lt_i32 vcc_lo, 0
s_cbranch_scc1 2b
s_mov_b32 s2, 0x100000
s_mov_b32 s3, 0x20000
s_movk_i32 s8, 0xff
s_branch .Lc_28
.Lc_27:
s_add_i32 s2, s2, -1
s_cmp_eq_u32 s2, 0
s_cselect_b64 s[0:1], -1, 0
s_andn2_b64 vcc, exec, s[0:1]
s_cbranch_vccz .Lc_30
.Lc_28:
buffer_load_dwordx4 v[0:3], v133, s[20:23], 0 offen sc1
buffer_load_dwordx4 v[4:7], v133, s[20:23], s3 offen sc1
s_waitcnt vmcnt(1)
v_xor_b32_e32 v0, v0, v134
s_waitcnt vmcnt(0)
v_xor_b32_e32 v4, v4, v134
v_xor_b32_e32 v5, v5, v135
v_xor_b32_e32 v7, v7, v135
v_xor_b32_e32 v1, v1, v135
v_xor_b32_e32 v2, v2, v134
v_xor_b32_e32 v3, v3, v135
v_xor_b32_e32 v6, v6, v134
v_add_f32_e32 v8, v0, v4
v_or3_b32 v4, v4, v5, v7
v_add_f32_e32 v9, v2, v6
v_add_f32_e32 v10, v1, v5
v_add_f32_e32 v11, v3, v7
v_or3_b32 v1, v4, v6, v1
v_permlane32_swap_b32_e32 v8, v9
v_permlane32_swap_b32_e32 v10, v11
v_or3_b32 v0, v1, v0, v3
v_add_f32_e32 v4, v8, v9
)ASM"
R"ASM(
v_add_f32_e32 v5, v10, v11
v_bitop3_b32 v0, v0, s8, v2 bitop3:0xc8
s_nop 0
v_permlane16_swap_b32_e32 v4, v5
v_cmp_ne_u32_e32 vcc, 0, v0
s_cbranch_vccnz .Lc_27
.Lc_30:
v_mov_b32_e32 v1, s5
v_mov_b32_e32 v0, s4
v_mov_b32_e32 v3, s7
v_mov_b32_e32 v2, s6
v_add_f32_e32 v4, v4, v5
s_nop 1
v_add_f32_dpp v4, v4, v4 row_ror:8 row_mask:0xf bank_mask:0xf bound_ctrl:1
s_mov_b64 s[2:3], exec
s_nop 0
v_add_f32_dpp v4, v4, v4 row_ror:4 row_mask:0xf bank_mask:0xf bound_ctrl:1
s_nop 1
v_add_f32_dpp v4, v4, v4 row_ror:2 row_mask:0xf bank_mask:0xf bound_ctrl:1
s_nop 1
v_add_f32_dpp v4, v4, v4 row_ror:1 row_mask:0xf bank_mask:0xf bound_ctrl:1
v_add_f32_e32 v4, v4, v132
v_cvt_pk_bf16_f32 v4, v4, s0
.Lc_31:
v_readfirstlane_b32 s4, v0
v_readfirstlane_b32 s5, v1
v_readfirstlane_b32 s6, v2
v_readfirstlane_b32 s7, v3
v_cmp_eq_u64_e32 vcc, s[4:5], v[0:1]
s_nop 0
v_cmp_eq_u64_e64 s[0:1], s[6:7], v[2:3]
s_and_b64 s[0:1], vcc, s[0:1]
s_and_saveexec_b64 s[0:1], s[0:1]
buffer_store_short v4, v129, s[4:7], 0 offen
s_xor_b64 exec, exec, s[0:1]
s_cbranch_execnz .Lc_31
s_mov_b64 exec, s[2:3]
s_endpgm
.Lc_33:
v_mov_b32_e32 v98, 0xfffff
.Lc_34:
buffer_load_dwordx4 v[84:87], v141, s[20:23], 0 offen sc1
buffer_load_dwordx4 v[88:91], v141, s[20:23], s11 offen sc1
buffer_load_dwordx4 v[92:95], v141, s[20:23], s12 offen sc1
buffer_load_dwordx4 v[102:105], v141, s[20:23], s13 offen sc1
v_subrev_co_u32_e32 v98, vcc, 1, v98
s_waitcnt vmcnt(3)
v_perm_b32 v101, v87, v85, s14
v_perm_b32 v96, v87, v85, s15
v_lshlrev_b32_e32 v82, 16, v84
v_and_b32_e32 v83, 0xffff0000, v84
v_lshlrev_b32_e32 v80, 16, v86
v_and_b32_e32 v81, 0xffff0000, v86
v_xor_b32_e32 v101, v101, v142
s_waitcnt vmcnt(2)
v_perm_b32 v106, v91, v89, s14
v_perm_b32 v97, v91, v89, s15
v_lshlrev_b32_e32 v84, 16, v88
v_and_b32_e32 v85, 0xffff0000, v88
v_lshlrev_b32_e32 v86, 16, v90
v_and_b32_e32 v87, 0xffff0000, v90
v_bitop3_b32 v101, v106, v101, v142 bitop3:0xde
s_waitcnt vmcnt(1)
v_perm_b32 v107, v95, v93, s14
v_perm_b32 v99, v95, v93, s15
v_lshlrev_b32_e32 v88, 16, v92
v_and_b32_e32 v89, 0xffff0000, v92
v_lshlrev_b32_e32 v90, 16, v94
v_and_b32_e32 v91, 0xffff0000, v94
v_bitop3_b32 v101, v107, v101, v142 bitop3:0xde
s_waitcnt vmcnt(0)
v_perm_b32 v108, v105, v103, s14
v_perm_b32 v100, v105, v103, s15
v_lshlrev_b32_e32 v92, 16, v102
v_and_b32_e32 v93, 0xffff0000, v102
v_lshlrev_b32_e32 v94, 16, v104
v_and_b32_e32 v95, 0xffff0000, v104
v_bitop3_b32 v101, v108, v101, v142 bitop3:0xde
s_nop 0
v_cmp_ne_u32_e64 s[2:3], 0, v101
s_cmp_eq_u64 s[2:3], 0
s_cselect_b64 s[2:3], -1, 0
s_or_b64 s[2:3], s[2:3], vcc
s_andn2_b64 vcc, exec, s[2:3]
s_cbranch_vccnz .Lc_34
s_branch .Lc_17
.Lc_35:
v_mov_b32_e32 v0, 0xfffff
.Lc_36:
buffer_load_dwordx2 v[130:131], v138, s[20:23], 0 offen sc1
v_subrev_co_u32_e32 v0, vcc, 1, v0
s_waitcnt vmcnt(0)
v_cmp_ne_u32_e64 s[0:1], v131, v128
s_cmp_eq_u64 s[0:1], 0
s_cselect_b64 s[0:1], -1, 0
s_or_b64 s[0:1], s[0:1], vcc
s_andn2_b64 vcc, exec, s[0:1]
s_cbranch_vccnz .Lc_36
s_branch .Lc_22
.Lk_f:
s_and_b32 s18, s16, 7
s_waitcnt lgkmcnt(0)
s_cmp_lt_u32 s18, 4
s_mov_b32 s14, s76
s_mov_b32 s15, s77
s_mov_b64 s[24:25], s[14:15]
s_mov_b32 s10, s72
s_mov_b32 s11, s73
s_mov_b64 s[20:21], s[10:11]
s_cselect_b64 s[10:11], -1, 0
s_cmp_gt_u32 s18, 3
s_mov_b32 s2, s64
s_mov_b32 s3, s65
s_mov_b32 s4, s66
s_mov_b32 s5, s67
s_mov_b32 s6, s68
s_mov_b32 s7, s69
s_mov_b32 s8, s70
s_mov_b32 s9, s71
s_mov_b32 s12, s74
s_mov_b32 s13, s75
s_cbranch_scc0 .Lf_2
s_mul_i32 s14, s18, 3
s_add_i32 s17, s14, 4
s_cbranch_execz .Lf_3
s_branch .Lf_4
.Lf_2:
.Lf_3:
s_lshl_b32 s17, s18, 2
.Lf_4:
s_ashr_i32 s14, s16, 3
v_and_b32_e32 v1, 63, v0
v_bfe_u32 v2, v0, 4, 2
s_and_b64 s[10:11], s[10:11], exec
s_cselect_b32 s10, 4, 3
s_mov_b32 s28, 0
s_load_dwordx2 s[4:5], s[0:1], 0x38
s_waitcnt lgkmcnt(0)
s_ashr_i32 s1, s16, 2
s_and_b32 s1, s1, -4
s_lshl_b32 s6, s17, 6
s_mul_i32 s1, s1, s10
s_lshl_b32 s0, s10, 3
s_add_i32 s2, s10, -1
s_add_i32 s3, s6, s1
s_cmp_lt_i32 s14, s0
s_cselect_b64 s[0:1], -1, 0
v_min_u32_e32 v3, s2, v2
v_lshl_add_u32 v3, v3, 2, s3
s_and_b64 s[2:3], s[0:1], exec
s_cselect_b32 s26, 0x1c0000, 0
s_add_i32 s3, s6, s16
s_and_b32 s2, s14, 7
v_and_b32_e32 v4, 56, v0
s_andn2_b32 s3, s3, 63
v_lshl_or_b32 v4, s2, 6, v4
s_movk_i32 s6, 0x700
v_mov_b32_e32 v5, s3
v_mad_u32_u24 v4, v4, s6, v5
v_and_b32_e32 v5, 7, v0
v_lshlrev_b32_e32 v6, 3, v5
v_or_b32_e32 v4, v4, v6
v_lshlrev_b32_e32 v45, 1, v4
v_and_b32_e32 v4, 8, v0
v_cmp_eq_u32_e32 vcc, 0, v4
v_lshlrev_b32_e32 v4, 1, v2
s_lshl_b32 s2, s2, 13
v_or3_b32 v4, v4, v6, s3
v_lshl_add_u32 v4, v4, 2, s2
v_add_u32_e32 v4, 0x11000, v4
v_mov_b32_e32 v6, 0x7ffffff0
s_and_b64 vcc, vcc, s[0:1]
s_mov_b32 s2, 0x11000
v_cndmask_b32_e32 v46, v6, v4, vcc
v_lshlrev_b32_e32 v4, 13, v5
v_lshlrev_b32_e32 v5, 2, v3
v_mov_b32_e32 v44, 0
v_add3_u32 v42, v5, v4, s2
ds_read_b32 v5, v44 offset:1392
s_lshl_b32 s0, s14, 9
v_lshlrev_b32_e32 v4, 4, v0
v_lshlrev_b32_e32 v3, 10, v3
s_and_b32 s0, s0, 0x200
v_and_b32_e32 v4, 0xf0, v4
v_or3_b32 v3, v3, s0, v4
v_cmp_gt_u32_e64 s[0:1], s10, v2
s_mov_b32 s29, s28
s_waitcnt lgkmcnt(0)
v_readfirstlane_b32 s8, v5
v_cndmask_b32_e64 v43, v6, v3, s[0:1]
s_mov_b32 s30, s28
s_mov_b32 s31, s28
v_mov_b64_e32 v[6:7], s[28:29]
v_mov_b64_e32 v[2:3], s[28:29]
v_mov_b64_e32 v[8:9], s[30:31]
v_mov_b64_e32 v[4:5], s[30:31]
s_and_b32 s25, s25, 0xffff
s_mov_b32 s27, 0x20000
s_setprio 1
s_add_i32 s2, s8, 0x8c
)ASM"
R"ASM(
s_branch 2f
1:
s_sleep 1
2:
s_memrealtime vcc
s_waitcnt lgkmcnt(0)
s_sub_i32 vcc_lo, vcc_lo, s2
s_cmp_lt_i32 vcc_lo, -8
s_cbranch_scc1 1b
s_cmp_lt_i32 vcc_lo, 0
s_cbranch_scc1 2b
s_movk_i32 s2, 0xe00
buffer_load_dwordx4 v[34:37], v45, s[24:27], 0 offen nt
buffer_load_dwordx4 v[38:41], v45, s[24:27], s2 offen nt
s_movk_i32 s2, 0x1c00
s_movk_i32 s3, 0x2a00
buffer_load_dwordx4 v[26:29], v45, s[24:27], s2 offen nt
buffer_load_dwordx4 v[30:33], v45, s[24:27], s3 offen nt
s_movk_i32 s2, 0x3800
s_movk_i32 s3, 0x4600
buffer_load_dwordx4 v[18:21], v45, s[24:27], s2 offen nt
buffer_load_dwordx4 v[22:25], v45, s[24:27], s3 offen nt
s_movk_i32 s2, 0x5400
s_movk_i32 s3, 0x6200
buffer_load_dwordx4 v[10:13], v45, s[24:27], s2 offen nt
buffer_load_dwordx4 v[14:17], v45, s[24:27], s3 offen nt
s_mov_b32 s6, 0x1000000
s_branch .Lf_6
.Lf_5:
s_andn2_b64 vcc, exec, s[2:3]
s_cbranch_vccz .Lf_8
.Lf_6:
ds_read_b32 v45, v44 offset:1280
s_waitcnt lgkmcnt(0)
v_readfirstlane_b32 s2, v45
s_cmp_lg_u32 s2, 0
s_mov_b64 s[2:3], -1
s_cbranch_scc1 .Lf_5
s_add_i32 s6, s6, -1
s_cmp_eq_u32 s6, 0
s_cselect_b64 s[2:3], -1, 0
s_sleep 1
s_branch .Lf_5
.Lf_8:
v_mov_b32_e32 v47, 0
ds_read_b32 v45, v47 offset:1284
v_lshlrev_b32_e32 v1, 4, v1
s_mov_b32 s23, 0x20000
ds_read_b128 v[48:51], v1 offset:2432
s_mov_b32 s22, 0xe1000
s_and_b32 s21, s21, 0xffff
s_waitcnt lgkmcnt(1)
v_and_b32_e32 v44, 0xff, v45
v_bfe_u32 v45, v45, 8, 8
s_waitcnt lgkmcnt(0)
s_mov_b32 s2, 0x5040100
s_mov_b32 s3, 0x7060302
s_waitcnt vmcnt(6)
v_perm_b32 v52, v38, v34, s2
v_mov_b32_e32 v53, 0
v_perm_b32 v34, v38, v34, s3
v_mov_b32_e32 v38, 0
v_dot2c_f32_bf16_e32 v53, v52, v48
v_dot2c_f32_bf16_e32 v38, v34, v48
v_perm_b32 v34, v39, v35, s2
v_mov_b32_e32 v52, 0
v_dot2c_f32_bf16_e32 v52, v34, v48
v_perm_b32 v34, v39, v35, s3
v_mov_b32_e32 v35, 0
v_dot2c_f32_bf16_e32 v35, v34, v48
v_perm_b32 v34, v40, v36, s2
v_mov_b32_e32 v39, 0
v_dot2c_f32_bf16_e32 v39, v34, v48
v_perm_b32 v34, v40, v36, s3
v_mov_b32_e32 v36, 0
v_dot2c_f32_bf16_e32 v36, v34, v48
v_perm_b32 v34, v41, v37, s2
v_mov_b32_e32 v40, 0
v_dot2c_f32_bf16_e32 v40, v34, v48
v_perm_b32 v34, v41, v37, s3
v_mov_b32_e32 v37, 0
v_dot2c_f32_bf16_e32 v37, v34, v48
s_waitcnt vmcnt(4)
v_perm_b32 v34, v30, v26, s2
v_perm_b32 v26, v30, v26, s3
v_dot2c_f32_bf16_e32 v38, v26, v49
v_perm_b32 v26, v31, v27, s2
v_dot2c_f32_bf16_e32 v52, v26, v49
v_perm_b32 v26, v31, v27, s3
v_dot2c_f32_bf16_e32 v35, v26, v49
v_perm_b32 v26, v32, v28, s2
v_dot2c_f32_bf16_e32 v39, v26, v49
v_perm_b32 v26, v32, v28, s3
v_dot2c_f32_bf16_e32 v36, v26, v49
v_perm_b32 v26, v33, v29, s2
v_dot2c_f32_bf16_e32 v40, v26, v49
v_perm_b32 v26, v33, v29, s3
v_dot2c_f32_bf16_e32 v53, v34, v49
v_dot2c_f32_bf16_e32 v37, v26, v49
s_waitcnt vmcnt(2)
v_perm_b32 v26, v22, v18, s2
v_perm_b32 v18, v22, v18, s3
v_dot2c_f32_bf16_e32 v38, v18, v50
v_perm_b32 v18, v23, v19, s2
v_dot2c_f32_bf16_e32 v52, v18, v50
v_perm_b32 v18, v23, v19, s3
v_dot2c_f32_bf16_e32 v35, v18, v50
v_perm_b32 v18, v24, v20, s2
v_dot2c_f32_bf16_e32 v39, v18, v50
v_perm_b32 v18, v24, v20, s3
v_dot2c_f32_bf16_e32 v36, v18, v50
v_perm_b32 v18, v25, v21, s2
v_dot2c_f32_bf16_e32 v40, v18, v50
v_perm_b32 v18, v25, v21, s3
v_dot2c_f32_bf16_e32 v53, v26, v50
v_dot2c_f32_bf16_e32 v37, v18, v50
s_waitcnt vmcnt(0)
v_perm_b32 v18, v14, v10, s2
v_perm_b32 v10, v14, v10, s3
v_dot2c_f32_bf16_e32 v38, v10, v51
v_perm_b32 v10, v15, v11, s2
v_dot2c_f32_bf16_e32 v52, v10, v51
v_perm_b32 v10, v15, v11, s3
v_dot2c_f32_bf16_e32 v35, v10, v51
v_perm_b32 v10, v16, v12, s2
v_dot2c_f32_bf16_e32 v39, v10, v51
v_perm_b32 v10, v16, v12, s3
v_dot2c_f32_bf16_e32 v36, v10, v51
v_perm_b32 v10, v17, v13, s2
v_dot2c_f32_bf16_e32 v40, v10, v51
v_perm_b32 v10, v17, v13, s3
v_dot2c_f32_bf16_e32 v53, v18, v51
v_dot2c_f32_bf16_e32 v37, v10, v51
s_nop 1
v_permlane32_swap_b32_e32 v53, v39
v_permlane32_swap_b32_e32 v38, v36
v_permlane32_swap_b32_e32 v52, v40
v_permlane32_swap_b32_e32 v35, v37
v_add_f32_e32 v10, v53, v39
v_add_f32_e32 v11, v38, v36
v_add_f32_e32 v12, v52, v40
v_add_f32_e32 v13, v35, v37
s_nop 0
v_permlane16_swap_b32_e32 v10, v12
v_permlane16_swap_b32_e32 v11, v13
v_add_f32_e32 v10, v10, v12
v_add_f32_e32 v11, v11, v13
s_movk_i32 s2, 0xff00
v_add_f32_dpp v10, v10, v10 row_ror:8 row_mask:0xf bank_mask:0xf bound_ctrl:1
v_add_f32_dpp v11, v11, v11 row_ror:8 row_mask:0xf bank_mask:0xf bound_ctrl:1
v_and_or_b32 v10, v10, s2, v44
v_and_or_b32 v11, v11, s2, v45
buffer_store_dwordx2 v[10:11], v46, s[20:23], 0 offen sc1
s_setprio 3
s_mov_b32 s6, 0x1000000
s_branch .Lf_10
.Lf_9:
s_andn2_b64 vcc, exec, s[2:3]
s_cbranch_vccz .Lf_12
.Lf_10:
ds_read_b128 v[10:13], v47 offset:1264
s_waitcnt lgkmcnt(0)
v_and_b32_e32 v10, v10, v11
v_and_b32_e32 v10, v10, v12
v_and_b32_e32 v10, v10, v13
s_nop 0
v_readfirstlane_b32 s2, v10
s_cmp_lg_u32 s2, 0
s_mov_b64 s[2:3], -1
s_cbranch_scc1 .Lf_9
s_add_i32 s6, s6, -1
s_cmp_eq_u32 s6, 0
s_cselect_b64 s[2:3], -1, 0
s_sleep 1
s_branch .Lf_9
.Lf_12:
s_and_b32 s5, s5, 0xffff
s_mov_b32 s7, 0x20000
s_mov_b32 s6, 0x1c0000
s_memrealtime s[2:3]
s_waitcnt lgkmcnt(0)
s_sub_i32 s2, s2, s8
s_max_i32 s2, s2, 0x134
s_add_i32 s2, s8, s2
s_add_i32 s3, s2, 12
s_branch 2f
1:
s_sleep 1
2:
s_memrealtime vcc
s_waitcnt lgkmcnt(0)
s_sub_i32 vcc_lo, vcc_lo, s3
s_cmp_lt_i32 vcc_lo, -8
s_cbranch_scc1 1b
s_cmp_lt_i32 vcc_lo, 0
s_cbranch_scc1 2b
s_movk_i32 s3, 0x100
buffer_load_dwordx4 v[14:17], v43, s[4:7], 0 offen nt
buffer_load_dwordx4 v[10:13], v43, s[4:7], s3 offen nt
s_movk_i32 s3, 0x400
)ASM"
R"ASM(
s_movk_i32 s9, 0x500
buffer_load_dwordx4 v[22:25], v43, s[4:7], s3 offen nt
buffer_load_dwordx4 v[18:21], v43, s[4:7], s9 offen nt
s_add_i32 s2, s2, 25
s_branch 2f
1:
s_sleep 1
2:
s_memrealtime vcc
s_waitcnt lgkmcnt(0)
s_sub_i32 vcc_lo, vcc_lo, s2
s_cmp_lt_i32 vcc_lo, -8
s_cbranch_scc1 1b
s_cmp_lt_i32 vcc_lo, 0
s_cbranch_scc1 2b
s_movk_i32 s2, 0x800
s_movk_i32 s3, 0x900
buffer_load_dwordx4 v[30:33], v43, s[4:7], s2 offen nt
buffer_load_dwordx4 v[26:29], v43, s[4:7], s3 offen nt
s_movk_i32 s2, 0xc00
s_movk_i32 s3, 0xd00
buffer_load_dwordx4 v[38:41], v43, s[4:7], s2 offen nt
buffer_load_dwordx4 v[34:37], v43, s[4:7], s3 offen nt
s_addk_i32 s8, 0x154
s_branch 2f
1:
s_sleep 1
2:
s_memrealtime vcc
s_waitcnt lgkmcnt(0)
s_sub_i32 vcc_lo, vcc_lo, s8
s_cmp_lt_i32 vcc_lo, -8
s_cbranch_scc1 1b
s_cmp_lt_i32 vcc_lo, 0
s_cbranch_scc1 2b
buffer_load_dwordx4 v[46:49], v42, s[20:23], 0 offen sc1
s_setprio 1
s_waitcnt vmcnt(0)
v_xor_b32_e32 v43, v46, v44
v_xor_b32_e32 v46, v47, v45
v_xor_b32_e32 v47, v48, v44
v_xor_b32_e32 v48, v49, v45
v_or3_b32 v49, v43, v46, v48
s_movk_i32 s4, 0xff
v_bitop3_b32 v49, v49, s4, v47 bitop3:0xc8
v_cmp_ne_u32_e32 vcc, 0, v49
s_cbranch_vccnz .Lf_14
.Lf_13:
v_add_f32_dpp v42, v43, v43 quad_perm:[1,0,3,2] row_mask:0xf bank_mask:0xf bound_ctrl:1
v_add_f32_dpp v43, v46, v46 quad_perm:[1,0,3,2] row_mask:0xf bank_mask:0xf bound_ctrl:1
v_and_b32_e32 v46, 1, v0
v_add_f32_dpp v44, v47, v47 quad_perm:[1,0,3,2] row_mask:0xf bank_mask:0xf bound_ctrl:1
v_add_f32_dpp v45, v48, v48 quad_perm:[1,0,3,2] row_mask:0xf bank_mask:0xf bound_ctrl:1
v_cmp_eq_u32_e32 vcc, 0, v46
v_and_b32_e32 v0, 2, v0
v_mov_b32_e32 v66, 1
v_cndmask_b32_e32 v42, v43, v42, vcc
v_cndmask_b32_e32 v43, v45, v44, vcc
v_cmp_eq_u32_e32 vcc, 0, v0
v_add_f32_dpp v42, v42, v42 quad_perm:[2,3,0,1] row_mask:0xf bank_mask:0xf bound_ctrl:1
v_add_f32_dpp v43, v43, v43 quad_perm:[2,3,0,1] row_mask:0xf bank_mask:0xf bound_ctrl:1
v_cndmask_b32_e32 v0, v43, v42, vcc
s_nop 1
v_add_f32_dpp v0, v0, v0 row_ror:4 row_mask:0xf bank_mask:0xf bound_ctrl:1
v_cvt_pk_bf16_f32 v0, v0, v0
v_lshlrev_b32_e32 v0, 16, v0
v_mul_f32_e32 v42, 0xbfb8aa3b, v0
v_exp_f32_e32 v42, v42
s_nop 0
v_add_f32_e32 v42, 1.0, v42
v_rcp_f32_e32 v42, v42
s_nop 0
v_mul_f32_e32 v0, v42, v0
v_cndmask_b32_e64 v0, 0, v0, s[0:1]
s_nop 1
v_mov_b32_dpp v42, v0 quad_perm:[1,0,3,2] row_mask:0xf bank_mask:0xf bound_ctrl:1
v_cvt_pk_bf16_f32 v0, v0, v42
s_nop 1
v_mov_b32_dpp v6, v0 quad_perm:[0,0,0,0] row_mask:0xf bank_mask:0x1
v_mov_b32_dpp v7, v0 quad_perm:[2,2,2,2] row_mask:0xf bank_mask:0x1
v_mov_b32_dpp v8, v0 quad_perm:[0,0,0,0] row_mask:0xf bank_mask:0x4
v_mov_b32_dpp v9, v0 quad_perm:[2,2,2,2] row_mask:0xf bank_mask:0x4
v_mov_b32_dpp v2, v0 quad_perm:[0,0,0,0] row_mask:0xf bank_mask:0x2
v_mov_b32_dpp v3, v0 quad_perm:[2,2,2,2] row_mask:0xf bank_mask:0x2
v_mov_b32_dpp v4, v0 quad_perm:[0,0,0,0] row_mask:0xf bank_mask:0x8
v_mov_b32_dpp v5, v0 quad_perm:[2,2,2,2] row_mask:0xf bank_mask:0x8
s_mov_b32 s0, 0x5040100
v_perm_b32 v42, v22, v14, s0
v_perm_b32 v43, v38, v30, s0
v_perm_b32 v44, v18, v10, s0
v_perm_b32 v45, v34, v26, s0
s_mov_b32 s1, 0x7060302
v_perm_b32 v46, v22, v14, s1
v_perm_b32 v47, v38, v30, s1
v_perm_b32 v48, v18, v10, s1
v_perm_b32 v49, v34, v26, s1
v_perm_b32 v50, v23, v15, s0
v_perm_b32 v51, v39, v31, s0
v_perm_b32 v52, v19, v11, s0
v_perm_b32 v53, v35, v27, s0
v_perm_b32 v54, v23, v15, s1
v_perm_b32 v55, v39, v31, s1
v_perm_b32 v56, v19, v11, s1
v_perm_b32 v57, v35, v27, s1
v_mfma_f32_16x16x32_bf16 v[42:45], v[6:9], v[42:45], 0
v_perm_b32 v58, v24, v16, s0
v_mfma_f32_16x16x32_bf16 v[46:49], v[6:9], v[46:49], 0
v_perm_b32 v59, v40, v32, s0
v_perm_b32 v60, v20, v12, s0
v_perm_b32 v61, v36, v28, s0
v_mfma_f32_16x16x32_bf16 v[50:53], v[6:9], v[50:53], 0
v_perm_b32 v62, v24, v16, s1
v_perm_b32 v63, v40, v32, s1
v_perm_b32 v64, v20, v12, s1
v_mfma_f32_16x16x32_bf16 v[6:9], v[6:9], v[54:57], 0
v_perm_b32 v65, v36, v28, s1
v_perm_b32 v54, v25, v17, s0
v_perm_b32 v55, v41, v33, s0
v_perm_b32 v56, v21, v13, s0
v_mfma_f32_16x16x32_bf16 v[42:45], v[2:5], v[58:61], v[42:45]
v_perm_b32 v57, v37, v29, s0
v_perm_b32 v10, v25, v17, s1
v_perm_b32 v11, v41, v33, s1
v_perm_b32 v12, v21, v13, s1
v_perm_b32 v13, v37, v29, s1
v_mov_b32_e32 v0, 0
v_mfma_f32_16x16x32_bf16 v[44:47], v[2:5], v[62:65], v[46:49]
v_mfma_f32_16x16x32_bf16 v[14:17], v[2:5], v[54:57], v[50:53]
v_mfma_f32_16x16x32_bf16 v[2:5], v[2:5], v[10:13], v[6:9]
s_nop 5
v_mov_b32_e32 v43, v44
v_mov_b32_e32 v44, v14
v_mov_b32_e32 v45, v2
ds_write_b128 v1, v[42:45] offset:1408
ds_write_b32 v0, v66 offset:1296
s_endpgm
.Lf_14:
v_mov_b32_e32 v49, 0xfffff
.Lf_15:
buffer_load_dwordx4 v[50:53], v42, s[20:23], 0 offen sc1
v_subrev_co_u32_e32 v49, vcc, 1, v49
s_waitcnt vmcnt(0)
v_xor_b32_e32 v43, v50, v44
v_xor_b32_e32 v46, v51, v45
v_xor_b32_e32 v48, v53, v45
v_xor_b32_e32 v47, v52, v44
v_or3_b32 v50, v43, v46, v48
v_bitop3_b32 v50, v50, s4, v47 bitop3:0xc8
v_cmp_ne_u32_e64 s[2:3], 0, v50
s_cmp_eq_u64 s[2:3], 0
s_cselect_b64 s[2:3], -1, 0
s_or_b64 s[2:3], s[2:3], vcc
s_andn2_b64 vcc, exec, s[2:3]
s_cbranch_vccnz .Lf_15
s_branch .Lf_13
.Lk_end:
)ASM"
    :: "{s[0:1]}"(__builtin_amdgcn_kernarg_segment_ptr()), "{s2}"(__builtin_amdgcn_workgroup_id_x()), "{v0}"(__builtin_amdgcn_workitem_id_x()),
       "s"((__attribute__((address_space(3))) unsigned char*)lds_)
    : "v1", "v2", "v3", "v4", "v5", "v6", "v7", "v8", "v9", "v10", "v11", "v12", "v13", "v14", "v15", "v16", "v17", "v18", "v19", "v20", "v21", "v22", "v23", "v24", "v25", "v26", "v27", "v28", "v29", "v30", "v31", "v32", "v33", "v34", "v35", "v36", "v37", "v38", "v39", "v40", "v41", "v42", "v43", "v44", "v45", "v46", "v47", "v48", "v49", "v50", "v51", "v52", "v53", "v54", "v55", "v56", "v57", "v58", "v59", "v60", "v61", "v62", "v63", "v64", "v65", "v66", "v67", "v68", "v69", "v70", "v71", "v72", "v73", "v74", "v75", "v76", "v77", "v78", "v79", "v80", "v81", "v82", "v83", "v84", "v85", "v86", "v87", "v88", "v89", "v90", "v91", "v92", "v93", "v94", "v95", "v96", "v97", "v98", "v99", "v100", "v101", "v102", "v103", "v104", "v105", "v106", "v107", "v108", "v109", "v110", "v111", "v112", "v113", "v114", "v115", "v116", "v117", "v118", "v119", "v120", "v121", "v122", "v123", "v124", "v125", "v126", "v127", "v128", "v129", "v130", "v131", "v132", "v133", "v134", "v135", "v136", "v137", "v138", "v139", "v140", "v141", "v142", "v143", "v144", "v145", "v146", "v147", "v148", "v149", "v150", "v151", "v152", "v153", "v154", "v155", "v156", "v157", "v158", "v159", "v160", "s3", "s4", "s5", "s6", "s7", "s8", "s9", "s10", "s11", "s12", "s13", "s14", "s15", "s16", "s17", "s18", "s19", "s20", "s21", "s22", "s23", "s24", "s25", "s26", "s27", "s28", "s29", "s30", "s31", "s33", "s34", "s35", "s36", "s37", "s38", "s39", "s40", "s41", "s42", "s43", "s44", "s45", "s46", "s47", "s48", "s49", "s50", "s51", "s52", "s53", "s54", "s55", "s56", "s57", "s58", "s59", "s60", "s61", "s62", "s63", "s64", "s65", "s66", "s67", "s68", "s69", "s70", "s71", "s72", "s73", "s74", "s75", "s76", "s77", "s78", "s79", "vcc", "memory");
}

extern "C" void kernel_launch(void* const* d_in, const int* in_sizes, int n_in, void* d_out, int out_size, void* d_ws, size_t ws_size, hipStream_t stream) {
  fused_decode<<<256, 448, 0, stream>>>((const u16*)d_in[0], (const u16*)d_in[1], (const u16*)d_in[2], (const u16*)d_in[3], (unsigned char*)d_ws + ((ws_size - (4u << 20)) & ~(size_t)4095),
                                        (const u16*)d_in[4], (const u16*)d_in[6], (const u16*)d_in[7], (u16*)d_out);
}
```
